```python
import torch

def _softmax(x, axis=-1):
    return torch.softmax(x, dim=axis)
def _gelu(x):
    return torch.nn.functional.gelu(x, approximate="tanh")
def _silu(x):
    return torch.nn.functional.silu(x)
def _sigmoid(x):
    return torch.sigmoid(x)
def _layernorm(x, axis=-1, eps=1e-5):
    m = x.mean(dim=axis, keepdim=True)
    v = x.var(dim=axis, keepdim=True, unbiased=False)
    return (x - m) / torch.sqrt(v + eps)
def _scatter_add(tgt, idx, src, axis=0):
    return tgt.clone().index_add_(axis, idx, src)
def _segment_sum(src, seg, n):
    out = torch.zeros((n,) + src.shape[1:], dtype=src.dtype)
    return out.index_add_(0, seg, src)
def _pad(x, pw):
    flat = tuple(v for pair in reversed(pw) for v in pair)
    return torch.nn.functional.pad(x, flat)

kernel_name = 'interleave_layers__sliding_block__glu_gated_bilinear__prompt'

def setup_inputs():

    _w = (lambda a: (lambda t: t.to(torch.bfloat16) if t.is_floating_point() else t)(torch.as_tensor(a)))
    def _A_setup():
        B, S, D = 1, 2048, 448
        return {
            "x": _w((lambda *s: torch.randn(*s, dtype=torch.bfloat16) * 0.05)(B, S, D)),
            "wqkv": _w((lambda *s: torch.randn(*s, dtype=torch.bfloat16) * 0.05)(D, 3 * D)),
            "wo": _w((lambda *s: torch.randn(*s, dtype=torch.bfloat16) * 0.05)(D, D)),
        }
    _A_in = _A_setup()

    def _B_setup():
        B, S, D, F = 1, 2048, 448, 1792
        return {
            "x": _w((lambda *s: torch.randn(*s, dtype=torch.bfloat16) * 0.05)(B, S, D)),
            "w_gate": _w((lambda *s: torch.randn(*s, dtype=torch.bfloat16) * 0.8)(D, F) * (D ** -0.5)),
            "w_up": _w((lambda *s: torch.randn(*s, dtype=torch.bfloat16) * 0.8)(D, F) * (D ** -0.5)),
            "w_down": _w((lambda *s: torch.randn(*s, dtype=torch.bfloat16) * 0.8)(F, D) * (F ** -0.5)),
        }
    _B_in = _B_setup()

    return {
        "A_x": _A_in["x"],
        "A_wqkv": _A_in["wqkv"],
        "A_wo": _A_in["wo"],
        "B_x": _B_in["x"],
        "B_w_gate": _B_in["w_gate"],
        "B_w_up": _B_in["w_up"],
        "B_w_down": _B_in["w_down"],
    }

def reference(A_x, A_wqkv, A_wo, B_x, B_w_gate, B_w_up, B_w_down):
    def _A_ref(x, wqkv, wo):
        B, S, D = x.shape
        H = 16; Dh = D // H; W = 256
        qkv = (lambda x, *d: x.permute(*d))((x @ wqkv).reshape(B, S, 3, H, Dh), 2, 0, 3, 1, 4)
        q, k, v = qkv[0], qkv[1], qkv[2]
        scores = torch.einsum("bhqd,bhkd->bhqk", q, k) * (Dh ** -0.5)
        idx = torch.arange(S)
        mask = (idx[:, None] - idx[None, :] >= 0) & (idx[:, None] - idx[None, :] < W)
        scores = torch.where(mask[None, None], scores, -1e9)
        p = _softmax(scores, axis=-1)
        o = torch.einsum("bhqk,bhkd->bhqd", p, v)
        return (lambda x, *d: x.permute(*d))(o, 0, 2, 1, 3).reshape(B, S, D) @ wo

    def _B_ref(x, w_gate, w_up, w_down):
        g = torch.einsum("bsd,df->bsf", x, w_gate)
        u = torch.einsum("bsd,df->bsf", x, w_up)
        h = g * u
        return torch.einsum("bsf,fd->bsd", h, w_down)

    x = A_x
    x = x + _A_ref(_layernorm(x, axis=-1), A_wqkv, A_wo)
    x = x + _B_ref(_layernorm(x, axis=-1), B_w_gate, B_w_up, B_w_down)
    x = x + _A_ref(_layernorm(x, axis=-1), A_wqkv, A_wo)
    x = x + _B_ref(_layernorm(x, axis=-1), B_w_gate, B_w_up, B_w_down)
    return x
```

```cpp
#include <hip/hip_runtime.h>
#include <hip/hip_bf16.h>
#include <stdint.h>

typedef __attribute__((ext_vector_type(8))) short s16x8;
typedef __attribute__((ext_vector_type(4))) short s16x4;
typedef __attribute__((ext_vector_type(4))) float f32x4;
typedef __attribute__((ext_vector_type(2))) float f32x2;
typedef __attribute__((ext_vector_type(2))) __bf16 bf16x2;
typedef __attribute__((ext_vector_type(4))) unsigned u32x4;
typedef __attribute__((ext_vector_type(2))) unsigned u32x2;
#define LAS __attribute__((address_space(3)))

static constexpr int S_ = 2048, D_ = 448, F_ = 1792, NH = 16, DH = 28;
static constexpr int KS = 14, KSF = 56;

static constexpr size_t OFF_WO = 0;
static constexpr size_t OFF_WGU = OFF_WO + 7 * 14 * 4 * 1024;
static constexpr size_t OFF_WD = OFF_WGU + 112 * 14 * 2 * 1024;
static constexpr size_t OFF_Q = OFF_WD + 7 * 56 * 4 * 1024;
static constexpr size_t OFF_K = OFF_Q + 16 * 128 * 1024;
static constexpr size_t OFF_V = OFF_K + 16 * 128 * 1024;
static constexpr size_t OFF_O = OFF_V + 16 * 128 * 1024;
static constexpr size_t OFF_X1 = OFF_O + 128 * 14 * 1024;
static constexpr size_t OFF_X2 = OFF_X1 + 128 * 14 * 1024;
static constexpr size_t OFF_H = OFF_X2 + 128 * 14 * 1024;

__device__ __forceinline__ float bf_lo(unsigned u) { return __uint_as_float(u << 16); }
__device__ __forceinline__ float bf_hi(unsigned u) { return __uint_as_float(u & 0xffff0000u); }
__device__ __forceinline__ unsigned pk_bf16(float a, float b) {
  f32x2 f = {a, b};
  bf16x2 r = __builtin_convertvector(f, bf16x2);
  return __builtin_bit_cast(unsigned, r);
}
__device__ __forceinline__ float rnd_bf16(float a) { return bf_lo(pk_bf16(a, 0.f)); }

__device__ __forceinline__ float red_add_g(float v) {
  auto p = __builtin_amdgcn_permlane16_swap(__float_as_uint(v), __float_as_uint(v), false, false);
  v = __uint_as_float(p[0]) + __uint_as_float(p[1]);
  auto q = __builtin_amdgcn_permlane32_swap(__float_as_uint(v), __float_as_uint(v), false, false);
  return __uint_as_float(q[0]) + __uint_as_float(q[1]);
}
__device__ __forceinline__ float red_max_g(float v) {
  auto p = __builtin_amdgcn_permlane16_swap(__float_as_uint(v), __float_as_uint(v), false, false);
  v = fmaxf(__uint_as_float(p[0]), __uint_as_float(p[1]));
  auto q = __builtin_amdgcn_permlane32_swap(__float_as_uint(v), __float_as_uint(v), false, false);
  return fmaxf(__uint_as_float(q[0]), __uint_as_float(q[1]));
}

__device__ __forceinline__ f32x4 mfma16(u32x4 a, u32x4 b, f32x4 c) {
  return __builtin_amdgcn_mfma_f32_16x16x32_bf16(__builtin_bit_cast(s16x8, a), __builtin_bit_cast(s16x8, b), c, 0, 0, 0);
}

__device__ __forceinline__ void ln_frag(u32x4 (&v)[KS]) {
  float sum = 0.f;
#pragma unroll
  for (int s = 0; s < KS; ++s)
#pragma unroll
    for (int d = 0; d < 4; ++d) sum += bf_lo(v[s][d]) + bf_hi(v[s][d]);
  sum = red_add_g(sum);
  const float mean = sum * (1.0f / D_);
  float sq = 0.f;
#pragma unroll
  for (int s = 0; s < KS; ++s)
#pragma unroll
    for (int d = 0; d < 4; ++d) {
      float a = bf_lo(v[s][d]) - mean, b = bf_hi(v[s][d]) - mean;
      sq = fmaf(a, a, sq);
      sq = fmaf(b, b, sq);
    }
  sq = red_add_g(sq);
  const float mb = rnd_bf16(mean);
  const float vb = rnd_bf16(sq * (1.0f / D_));
  const float den = rnd_bf16(sqrtf(rnd_bf16(vb + 1e-5f)));
  const float inv = 1.0f / den;
#pragma unroll
  for (int s = 0; s < KS; ++s)
#pragma unroll
    for (int d = 0; d < 4; ++d) {
      unsigned dd = pk_bf16(bf_lo(v[s][d]) - mb, bf_hi(v[s][d]) - mb);
      v[s][d] = pk_bf16(bf_lo(dd) * inv, bf_hi(dd) * inv);
    }
}

__device__ __forceinline__ void pretile_task(int task, const uint16_t* __restrict__ wo, const uint16_t* __restrict__ wg,
                                             const uint16_t* __restrict__ wu, const uint16_t* __restrict__ wd,
                                             char* __restrict__ ws, int lane) {
  const int i = lane & 15, g = lane >> 4;
  const uint16_t* src;
  int ld, col0;
  int rows[8];
  char* dst;
  int jjstride;
  if (task < 98) {
    int nblk = task / 14, s = task % 14;
    src = wo; ld = D_; col0 = nblk * 64;
#pragma unroll
    for (int e = 0; e < 8; ++e) rows[e] = 32 * s + 8 * g + e;
    dst = ws + OFF_WO + (size_t)((nblk * 14 + s) * 4) * 1024; jjstride = 1024;
  } else if (task < 98 + 784) {
    int t = task - 98;
    int which = t & 1; t >>= 1;
    int blk = t / 14, s = t % 14;
    src = which ? wu : wg; ld = F_; col0 = blk * 64;
#pragma unroll
    for (int e = 0; e < 8; ++e) rows[e] = 32 * s + 8 * g + e;
    dst = ws + OFF_WGU + (size_t)(((blk * 4) * 14 + s) * 2 + which) * 1024; jjstride = 14 * 2 * 1024;
  } else {
    int t = task - 98 - 784;
    int nblk = t / 56, s = t % 56;
    src = wd; ld = D_; col0 = nblk * 64;
#pragma unroll
    for (int e = 0; e < 8; ++e) {
      int Fi = 2 * s + (e >> 2);
      rows[e] = 64 * (Fi >> 2) + 16 * g + 4 * (e & 3) + (Fi & 3);
    }
    dst = ws + OFF_WD + (size_t)((nblk * 56 + s) * 4) * 1024; jjstride = 1024;
  }
  u32x2 v[8];
#pragma unroll
  for (int e = 0; e < 8; ++e) v[e] = *(const u32x2*)(src + (size_t)rows[e] * ld + col0 + 4 * i);
#pragma unroll
  for (int jj = 0; jj < 4; ++jj) {
    u32x4 o;
#pragma unroll
    for (int d = 0; d < 4; ++d)
      o[d] = __builtin_amdgcn_perm(v[2 * d + 1][jj >> 1], v[2 * d][jj >> 1], (jj & 1) ? 0x07060302u : 0x05040100u);
    *(u32x4*)(dst + (size_t)jj * jjstride + lane * 16) = o;
  }
}
static constexpr int N_PRETILE = 98 + 784 + 392;

template <bool FIRST>
__global__ __launch_bounds__(512, 2) void k1_ln_qkv(const uint16_t* __restrict__ x, const uint16_t* __restrict__ wqkv,
                                                    const uint16_t* __restrict__ wo, const uint16_t* __restrict__ wg,
                                                    const uint16_t* __restrict__ wu, const uint16_t* __restrict__ wd,
                                                    char* __restrict__ ws) {
  extern __shared__ __attribute__((aligned(16))) char smem[];
  const int tid = threadIdx.x, lane = tid & 63, wave = __builtin_amdgcn_readfirstlane(tid >> 6);
  const int b = blockIdx.x;
  const bool gemm = b < 224;
  const int xcd = b & 7, r = b >> 3;
  const int panel = xcd * 2 + (r / 14), nt = r % 14;
  const int n0 = nt * 96;
  if (wave >= 4) {
    if (gemm) {
      const int w4 = wave - 4;
      int offs[3];
#pragma unroll
      for (int m = 0; m < 3; ++m) {
        int c = m * 64 + lane;
        int row = c / 12, chp = c % 12;
        int pp = chp >> 1;
        int sp = pp - ((row >> 3) & 1);
        if (sp < 0) sp += 6;
        offs[m] = row * (3 * D_ * 2) + sp * 32 + (chp & 1) * 16;
      }
      const char* wbase = (const char*)wqkv + n0 * 2;
#pragma unroll
      for (int it = 0; it < 7; ++it) {
        int grp = w4 * 7 + it;
#pragma unroll
        for (int m = 0; m < 3; ++m)
          __builtin_amdgcn_global_load_lds((const unsigned*)(wbase + (size_t)grp * 16 * (3 * D_ * 2) + offs[m]),
                                           (LAS unsigned*)(smem + (grp * 3 + m) * 1024), 16, 0, 0);
      }
    }
    if (FIRST) {
      for (int task = b * 4 + (wave - 4); task < N_PRETILE; task += 256 * 4) pretile_task(task, wo, wg, wu, wd, ws, lane);
    }
    if (gemm) {
      asm volatile("s_waitcnt vmcnt(0)" ::: "memory");
      __syncthreads();
    }
    return;
  }
  if (!gemm) return;
  const int tok = lane & 15, g = lane >> 4;
  u32x4 a[2][KS];
  const int tf0 = panel * 8 + wave * 2;
#pragma unroll
  for (int t = 0; t < 2; ++t)
#pragma unroll
    for (int s = 0; s < KS; ++s) {
      if (FIRST) a[t][s] = *(const u32x4*)(x + (size_t)((tf0 + t) * 16 + tok) * D_ + 32 * s + 8 * g);
      else a[t][s] = *(const u32x4*)((const char*)x + (size_t)(((tf0 + t) * KS + s) * 64 + lane) * 16);
    }
  ln_frag(a[0]);
  ln_frag(a[1]);
  __syncthreads();
  const int q4 = (lane & 15) >> 2, p4 = lane & 3;
  const int rowl = 8 * g + q4;
  const int rot = g & 1;
  f32x4 acc[6][2];
#pragma unroll
  for (int c = 0; c < 6; ++c) { acc[c][0] = f32x4{0, 0, 0, 0}; acc[c][1] = f32x4{0, 0, 0, 0}; }
  int cofs[6];
#pragma unroll
  for (int c = 0; c < 6; ++c) { int pp = c + rot; if (pp >= 6) pp -= 6; cofs[c] = rowl * 192 + pp * 32 + p4 * 8; }
#pragma unroll
  for (int s = 0; s < KS; ++s) {
#pragma unroll
    for (int c = 0; c < 6; ++c) {
      s16x4 lo = __builtin_amdgcn_ds_read_tr16_b64_v4i16((LAS s16x4*)(smem + cofs[c] + s * 32 * 192));
      s16x4 hi = __builtin_amdgcn_ds_read_tr16_b64_v4i16((LAS s16x4*)(smem + cofs[c] + s * 32 * 192 + 4 * 192));
      u32x2 l2 = __builtin_bit_cast(u32x2, lo), h2 = __builtin_bit_cast(u32x2, hi);
      u32x4 wf = {l2[0], l2[1], h2[0], h2[1]};
      acc[c][0] = mfma16(wf, a[0][s], acc[c][0]);
      acc[c][1] = mfma16(wf, a[1][s], acc[c][1]);
    }
  }
#pragma unroll
  for (int c = 0; c < 6; ++c) {
    const int n = n0 + 16 * c + 4 * g;
    const int part = n / D_, within = n - part * D_;
    const int head = within / DH, dh0 = within - head * DH;
    const int ch = dh0 >> 3;
    int inoff = (part == 2) ? (ch * 256 + ((tok * 16 + (ch & 1) * 128) & 255)) : (ch * 256 + tok * 16);
    inoff += (dh0 & 7) * 2;
    char* base = ws + OFF_Q + (size_t)part * (16 * 128 * 1024) + (size_t)(head * 128 + tf0) * 1024 + inoff;
#pragma unroll
    for (int t = 0; t < 2; ++t) {
      unsigned w0 = pk_bf16(acc[c][t][0], acc[c][t][1]), w1 = pk_bf16(acc[c][t][2], acc[c][t][3]);
      if (dh0 == 24) {
        u32x4 o = {w0, w1, (part == 2) ? 0x00003f80u : 0u, 0u};
        *(u32x4*)(base + t * 1024) = o;
      } else {
        u32x2 o = {w0, w1};
        *(u32x2*)(base + t * 1024) = o;
      }
    }
  }
}

template <bool MASKALL>
__device__ __forceinline__ void attn_body(const char* smem, u32x4 qf, int w, int lane, int p, char* __restrict__ optr_base,
                                          int head) {
  const int tok = lane & 15, g = lane >> 4;
  const int t0 = w & ~1;
  const char* kl = smem + t0 * 1024 + lane * 16;
  f32x4 sc[18];
#pragma unroll
  for (int i = 0; i < 18; ++i) {
    u32x4 kf = *(const u32x4*)(kl + i * 1024);
    sc[i] = mfma16(kf, qf, f32x4{0, 0, 0, 0});
  }
  const int dq = 16 * (16 + w) + tok - (16 * t0 + 4 * g);
  const int kneg = 16 * (16 - 8 * p);
  const float NEG = -INFINITY;
#pragma unroll
  for (int i = 0; i < 18; ++i) {
    if (MASKALL || i < 2 || i >= 16) {
#pragma unroll
      for (int r = 0; r < 4; ++r) {
        int d = dq - 16 * i - r;
        bool ok = (unsigned)d < 256u;
        if (MASKALL) ok = ok && (16 * (t0 + i) + 4 * g + r >= kneg);
        sc[i][r] = ok ? sc[i][r] : NEG;
      }
    }
  }
  float m = NEG;
#pragma unroll
  for (int i = 0; i < 18; ++i) m = fmaxf(m, fmaxf(fmaxf(sc[i][0], sc[i][1]), fmaxf(sc[i][2], sc[i][3])));
  m = red_max_g(m);
  const float cs = 0.18898223650461362f * 1.4426950408889634f;
  const float mc = m * cs;
  u32x4 pf[9];
#pragma unroll
  for (int i = 0; i < 18; ++i) {
    float e0 = __builtin_amdgcn_exp2f(fmaf(sc[i][0], cs, -mc));
    float e1 = __builtin_amdgcn_exp2f(fmaf(sc[i][1], cs, -mc));
    float e2 = __builtin_amdgcn_exp2f(fmaf(sc[i][2], cs, -mc));
    float e3 = __builtin_amdgcn_exp2f(fmaf(sc[i][3], cs, -mc));
    pf[i >> 1][(i & 1) * 2 + 0] = pk_bf16(e0, e1);
    pf[i >> 1][(i & 1) * 2 + 1] = pk_bf16(e2, e3);
  }
  const int q4 = (lane & 15) >> 2, p4 = lane & 3;
  const char* vl = smem + 24 * 1024 + t0 * 1024;
  int vofs[2];
#pragma unroll
  for (int f = 0; f < 2; ++f) {
    int c = 2 * f + (p4 >> 1);
    vofs[f] = c * 256 + (((4 * g + q4) * 16 + (c & 1) * 128) & 255) + (p4 & 1) * 8;
  }
  f32x4 o[2] = {f32x4{0, 0, 0, 0}, f32x4{0, 0, 0, 0}};
#pragma unroll
  for (int bb = 0; bb < 9; ++bb) {
#pragma unroll
    for (int f = 0; f < 2; ++f) {
      s16x4 lo = __builtin_amdgcn_ds_read_tr16_b64_v4i16((LAS s16x4*)(vl + vofs[f] + (2 * bb) * 1024));
      s16x4 hi = __builtin_amdgcn_ds_read_tr16_b64_v4i16((LAS s16x4*)(vl + vofs[f] + (2 * bb + 1) * 1024));
      u32x2 l2 = __builtin_bit_cast(u32x2, lo), h2 = __builtin_bit_cast(u32x2, hi);
      u32x4 vf = {l2[0], l2[1], h2[0], h2[1]};
      o[f] = mfma16(vf, pf[bb], o[f]);
    }
  }
  const float lsum = __shfl(o[1][0], 48 + tok);
  const float inv = 1.0f / lsum;
#pragma unroll
  for (int f = 0; f < 2; ++f) {
    if (f == 0 || g < 3) {
      const int k0 = DH * head + 16 * f + 4 * g;
      u32x2 st = {pk_bf16(o[f][0] * inv, o[f][1] * inv), pk_bf16(o[f][2] * inv, o[f][3] * inv)};
      *(u32x2*)(optr_base + (size_t)((k0 >> 5) * 64 + ((k0 & 31) >> 3) * 16 + tok) * 16 + (k0 & 7) * 2) = st;
    }
  }
}

__global__ __launch_bounds__(512, 2) void k2_attn(char* __restrict__ ws) {
  extern __shared__ __attribute__((aligned(16))) char smem[];
  const int tid = threadIdx.x, lane = tid & 63, wave = __builtin_amdgcn_readfirstlane(tid >> 6);
  const int b = blockIdx.x;
  const int xcd = b & 7, r = b >> 3;
  const int p = xcd * 2 + (r >> 4), head = r & 15;
  const char* kb = ws + OFF_K + (size_t)head * 128 * 1024;
  const char* vb = ws + OFF_V + (size_t)head * 128 * 1024;
#pragma unroll
  for (int j = 0; j < 3; ++j) {
    const int i = wave * 3 + j;
    const int T = p * 8 - 16 + i;
    if (T >= 0) {
      __builtin_amdgcn_global_load_lds((const unsigned*)(kb + (size_t)T * 1024 + lane * 16), (LAS unsigned*)(smem + i * 1024), 16, 0, 0);
      __builtin_amdgcn_global_load_lds((const unsigned*)(vb + (size_t)T * 1024 + lane * 16), (LAS unsigned*)(smem + (24 + i) * 1024), 16, 0, 0);
    } else {
      *(u32x4*)(smem + i * 1024 + lane * 16) = u32x4{0, 0, 0, 0};
      *(u32x4*)(smem + (24 + i) * 1024 + lane * 16) = u32x4{0, 0, 0, 0};
    }
  }
  const int tq = p * 8 + wave;
  u32x4 qf = *(const u32x4*)(ws + OFF_Q + (size_t)(head * 128 + tq) * 1024 + lane * 16);
  asm volatile("s_waitcnt vmcnt(0)" ::: "memory");
  __syncthreads();
  char* ob = ws + OFF_O + (size_t)tq * KS * 1024;
  if (p >= 2) attn_body<false>(smem, qf, wave, lane, p, ob, head);
  else attn_body<true>(smem, qf, wave, lane, p, ob, head);
}

template <int NKS, bool RES_RM, bool OUT_RM>
__global__ __launch_bounds__(256, 1) void k35_proj(const char* __restrict__ A, const char* __restrict__ Wt,
                                                   const char* __restrict__ res, char* __restrict__ out) {
  extern __shared__ __attribute__((aligned(16))) char smem[];
  const int tid = threadIdx.x, lane = tid & 63, wave = __builtin_amdgcn_readfirstlane(tid >> 6);
  const int b = blockIdx.x;
  const int xcd = b & 7, r = b >> 3;
  const int mt = xcd * 4 + (r / 7), nblk = r % 7;
  const int tok = lane & 15, g = lane >> 4;
  constexpr int NIT = (NKS + 3) / 4;
  constexpr int DEPTH = NIT < 5 ? NIT : 5;
  const char* ap = A + (size_t)(mt * 4 * NKS) * 1024 + lane * 16;
  const char* wp = Wt + (size_t)(nblk * NKS * 4) * 1024 + lane * 16;
  u32x4 af[DEPTH][4], wf[DEPTH][4];
  auto sidx = [&](int it) { return (NKS == 56) ? (wave * 14 + it) : (wave + 4 * it); };
  auto issue = [&](int it) {
    int s = sidx(it);
    if (NKS != 56 && s >= NKS) s = wave;
#pragma unroll
    for (int t = 0; t < 4; ++t) af[it % DEPTH][t] = *(const u32x4*)(ap + (size_t)(t * NKS + s) * 1024);
#pragma unroll
    for (int j = 0; j < 4; ++j) wf[it % DEPTH][j] = *(const u32x4*)(wp + (size_t)(s * 4 + j) * 1024);
  };
#pragma unroll
  for (int it = 0; it < DEPTH; ++it) issue(it);
  const int tf = mt * 4 + wave;
  u32x4 rs[2];
  size_t roff[2];
#pragma unroll
  for (int h = 0; h < 2; ++h) {
    size_t tiled = (size_t)((tf * KS + 2 * nblk + (g >> 1)) * 64 + (2 * (g & 1) + h) * 16 + tok) * 16;
    size_t rm = ((size_t)(tf * 16 + tok) * D_ + nblk * 64 + 16 * g + 8 * h) * 2;
    rs[h] = *(const u32x4*)(res + (RES_RM ? rm : tiled));
    roff[h] = OUT_RM ? rm : tiled;
  }
  f32x4 acc[4][4];
#pragma unroll
  for (int j = 0; j < 4; ++j)
#pragma unroll
    for (int t = 0; t < 4; ++t) acc[j][t] = f32x4{0, 0, 0, 0};
#pragma unroll
  for (int it = 0; it < NIT; ++it) {
    if (NKS == 56 || sidx(it) < NKS) {
#pragma unroll
      for (int j = 0; j < 4; ++j)
#pragma unroll
        for (int t = 0; t < 4; ++t) acc[j][t] = mfma16(wf[it % DEPTH][j], af[it % DEPTH][t], acc[j][t]);
    }
    if (it + DEPTH < NIT) issue(it + DEPTH);
  }
#pragma unroll
  for (int t = 0; t < 4; ++t) {
    if (t != wave) {
#pragma unroll
      for (int j = 0; j < 4; ++j) *(f32x4*)(smem + ((t * 4 + wave) * 4 + j) * 1024 + lane * 16) = acc[j][t];
    }
  }
  __syncthreads();
  f32x4 fin[4];
#pragma unroll
  for (int j = 0; j < 4; ++j) {
    fin[j] = wave == 0 ? acc[j][0] : wave == 1 ? acc[j][1] : wave == 2 ? acc[j][2] : acc[j][3];
  }
#pragma unroll
  for (int sw = 0; sw < 4; ++sw) {
    if (sw != wave) {
#pragma unroll
      for (int j = 0; j < 4; ++j) fin[j] += *(const f32x4*)(smem + ((wave * 4 + sw) * 4 + j) * 1024 + lane * 16);
    }
  }
#pragma unroll
  for (int h = 0; h < 2; ++h) {
    u32x4 o;
#pragma unroll
    for (int d = 0; d < 4; ++d) {
      const int c0 = 8 * h + 2 * d;
      unsigned y = pk_bf16(fin[c0 & 3][c0 >> 2], fin[(c0 + 1) & 3][(c0 + 1) >> 2]);
      o[d] = pk_bf16(bf_lo(rs[h][d]) + bf_lo(y), bf_hi(rs[h][d]) + bf_hi(y));
    }
    *(u32x4*)(out + roff[h]) = o;
  }
}

__global__ __launch_bounds__(512, 2) void k4_gu(char* __restrict__ ws) {
  extern __shared__ __attribute__((aligned(16))) char smem[];
  const int tid = threadIdx.x, lane = tid & 63, wave = __builtin_amdgcn_readfirstlane(tid >> 6);
  const int b = blockIdx.x;
  const int xcd = b & 7, r = b >> 3;
  const int panel = xcd * 2 + (r >> 4), j = r & 15;
  {
    u32x4 a[KS];
    const char* xp = ws + OFF_X1 + (size_t)((panel * 8 + wave) * KS) * 1024 + lane * 16;
#pragma unroll
    for (int s = 0; s < KS; ++s) a[s] = *(const u32x4*)(xp + s * 1024);
    const int Fi = j * 7 + (wave < 7 ? wave : 0);
    const char* wp = ws + OFF_WGU + (size_t)(Fi * KS * 2) * 1024 + lane * 16;
    u32x4 wf[KS][2];
#pragma unroll
    for (int s = 0; s < KS; ++s) {
      wf[s][0] = *(const u32x4*)(wp + (s * 2) * 1024);
      wf[s][1] = *(const u32x4*)(wp + (s * 2 + 1) * 1024);
    }
    ln_frag(a);
#pragma unroll
    for (int s = 0; s < KS; ++s) *(u32x4*)(smem + (wave * KS + s) * 1024 + lane * 16) = a[s];
    __syncthreads();
    if (wave == 7) return;
    f32x4 acc[8][2];
#pragma unroll
    for (int t = 0; t < 8; ++t) { acc[t][0] = f32x4{0, 0, 0, 0}; acc[t][1] = f32x4{0, 0, 0, 0}; }
#pragma unroll
    for (int s = 0; s < KS; ++s) {
#pragma unroll
      for (int t = 0; t < 8; ++t) {
        u32x4 xf = *(const u32x4*)(smem + (t * KS + s) * 1024 + lane * 16);
        acc[t][0] = mfma16(wf[s][0], xf, acc[t][0]);
        acc[t][1] = mfma16(wf[s][1], xf, acc[t][1]);
      }
    }
    char* hp = ws + OFF_H + (size_t)((panel * 8) * KSF + (Fi >> 1)) * 1024 + lane * 16 + (Fi & 1) * 8;
#pragma unroll
    for (int t = 0; t < 8; ++t) {
      unsigned g0 = pk_bf16(acc[t][0][0], acc[t][0][1]), g1 = pk_bf16(acc[t][0][2], acc[t][0][3]);
      unsigned u0 = pk_bf16(acc[t][1][0], acc[t][1][1]), u1 = pk_bf16(acc[t][1][2], acc[t][1][3]);
      u32x2 o = {pk_bf16(bf_lo(g0) * bf_lo(u0), bf_hi(g0) * bf_hi(u0)), pk_bf16(bf_lo(g1) * bf_lo(u1), bf_hi(g1) * bf_hi(u1))};
      *(u32x2*)(hp + (size_t)t * KSF * 1024) = o;
    }
  }
}

extern "C" void kernel_launch(void* const* d_in, const int* in_sizes, int n_in, void* d_out, int out_size, void* d_ws,
                              size_t ws_size, hipStream_t stream) {
  const uint16_t* x = (const uint16_t*)d_in[0];
  const uint16_t* wqkv = (const uint16_t*)d_in[1];
  const uint16_t* wo = (const uint16_t*)d_in[2];
  const uint16_t* wg = (const uint16_t*)d_in[4];
  const uint16_t* wu = (const uint16_t*)d_in[5];
  const uint16_t* wd = (const uint16_t*)d_in[6];
  char* ws = (char*)d_ws;
  constexpr int L1 = 448 * 192, L2 = 48 * 1024, L3 = 64 * 1024, L4 = 8 * KS * 1024;
  hipFuncSetAttribute((const void*)k1_ln_qkv<true>, hipFuncAttributeMaxDynamicSharedMemorySize, L1);
  hipFuncSetAttribute((const void*)k1_ln_qkv<false>, hipFuncAttributeMaxDynamicSharedMemorySize, L1);
  hipFuncSetAttribute((const void*)k4_gu, hipFuncAttributeMaxDynamicSharedMemorySize, L4);
  k1_ln_qkv<true><<<256, 512, L1, stream>>>(x, wqkv, wo, wg, wu, wd, ws);
  k2_attn<<<256, 512, L2, stream>>>(ws);
  k35_proj<14, true, false><<<224, 256, L3, stream>>>(ws + OFF_O, ws + OFF_WO, (const char*)x, ws + OFF_X1);
  k4_gu<<<256, 512, L4, stream>>>(ws);
  k35_proj<56, false, false><<<224, 256, L3, stream>>>(ws + OFF_H, ws + OFF_WD, ws + OFF_X1, ws + OFF_X2);
  k1_ln_qkv<false><<<224, 512, L1, stream>>>((const uint16_t*)(ws + OFF_X2), wqkv, wo, wg, wu, wd, ws);
  k2_attn<<<256, 512, L2, stream>>>(ws);
  k35_proj<14, false, false><<<224, 256, L3, stream>>>(ws + OFF_O, ws + OFF_WO, ws + OFF_X2, ws + OFF_X1);
  k4_gu<<<256, 512, L4, stream>>>(ws);
  k35_proj<56, false, true><<<224, 256, L3, stream>>>(ws + OFF_H, ws + OFF_WD, ws + OFF_X1, (char*)d_out);
}
```

```cpp
#include <hip/hip_runtime.h>
#include <hip/hip_bf16.h>
#include <stdint.h>

typedef __attribute__((ext_vector_type(8))) short s16x8;
typedef __attribute__((ext_vector_type(4))) short s16x4;
typedef __attribute__((ext_vector_type(4))) float f32x4;
typedef __attribute__((ext_vector_type(2))) float f32x2;
typedef __attribute__((ext_vector_type(2))) __bf16 bf16x2;
typedef __attribute__((ext_vector_type(4))) unsigned u32x4;
typedef __attribute__((ext_vector_type(2))) unsigned u32x2;
#define LAS __attribute__((address_space(3)))
typedef __attribute__((address_space(1))) unsigned gu32;

static constexpr int S_ = 2048, D_ = 448, F_ = 1792, NH = 16, DH = 28;
static constexpr int KS = 14, KSF = 56;

static constexpr unsigned OFF_CNT = 0;
static constexpr unsigned OFF_KVF = 32768;
static constexpr unsigned OFF_TMO = 98304;
static constexpr unsigned FLAG_BYTES = 98304 + 1024;
static constexpr unsigned OFF_W1 = 131072;
static constexpr unsigned OFF_CS1 = OFF_W1 + 16 * 14 * 6 * 1024;
static constexpr unsigned OFF_WO = OFF_CS1 + 8192;
static constexpr unsigned OFF_WGU = OFF_WO + 7 * 14 * 4 * 1024;
static constexpr unsigned OFF_CSGU = OFF_WGU + 112 * 14 * 2 * 1024;
static constexpr unsigned OFF_WD = OFF_CSGU + 16384;
static constexpr unsigned XT_BYTES = 128 * 14 * 1024;
static constexpr unsigned OFF_X = OFF_WD + 7 * 56 * 4 * 1024;
static constexpr unsigned OFF_X1 = OFF_X + 2 * XT_BYTES;
static constexpr unsigned OFF_O = OFF_X1 + 2 * XT_BYTES;
static constexpr unsigned KV_BYTES = 16 * 128 * 1024;
static constexpr unsigned OFF_K = OFF_O + 2 * XT_BYTES;
static constexpr unsigned OFF_V = OFF_K + 2 * KV_BYTES;
static constexpr unsigned H_BYTES = 128 * 56 * 1024;
static constexpr unsigned OFF_H = OFF_V + 2 * KV_BYTES;
static constexpr unsigned ST_BYTES = 2048 * 64;
static constexpr unsigned OFF_STX = OFF_H + 2 * H_BYTES;
static constexpr unsigned OFF_STX1 = OFF_STX + 2 * ST_BYTES;

__device__ __forceinline__ float bf_lo(unsigned u) { return __uint_as_float(u << 16); }
__device__ __forceinline__ float bf_hi(unsigned u) { return __uint_as_float(u & 0xffff0000u); }
__device__ __forceinline__ unsigned pk_bf16(float a, float b) {
  f32x2 f = {a, b};
  bf16x2 r = __builtin_convertvector(f, bf16x2);
  return __builtin_bit_cast(unsigned, r);
}
__device__ __forceinline__ float rnd_bf16(float a) { return bf_lo(pk_bf16(a, 0.f)); }
__device__ __forceinline__ float red_add_g(float v) {
  auto p = __builtin_amdgcn_permlane16_swap(__float_as_uint(v), __float_as_uint(v), false, false);
  v = __uint_as_float(p[0]) + __uint_as_float(p[1]);
  auto q = __builtin_amdgcn_permlane32_swap(__float_as_uint(v), __float_as_uint(v), false, false);
  return __uint_as_float(q[0]) + __uint_as_float(q[1]);
}
__device__ __forceinline__ float red_max_g(float v) {
  auto p = __builtin_amdgcn_permlane16_swap(__float_as_uint(v), __float_as_uint(v), false, false);
  v = fmaxf(__uint_as_float(p[0]), __uint_as_float(p[1]));
  auto q = __builtin_amdgcn_permlane32_swap(__float_as_uint(v), __float_as_uint(v), false, false);
  return fmaxf(__uint_as_float(q[0]), __uint_as_float(q[1]));
}
__device__ __forceinline__ f32x4 mfma16(u32x4 a, u32x4 b, f32x4 c) {
  return __builtin_amdgcn_mfma_f32_16x16x32_bf16(__builtin_bit_cast(s16x8, a), __builtin_bit_cast(s16x8, b), c, 0, 0, 0);
}
__device__ __forceinline__ u32x4 tr_frag(const char* p0, const char* p1) {
  s16x4 lo = __builtin_amdgcn_ds_read_tr16_b64_v4i16((LAS s16x4*)p0);
  s16x4 hi = __builtin_amdgcn_ds_read_tr16_b64_v4i16((LAS s16x4*)p1);
  u32x2 l2 = __builtin_bit_cast(u32x2, lo), h2 = __builtin_bit_cast(u32x2, hi);
  return u32x4{l2[0], l2[1], h2[0], h2[1]};
}
#define SB() __builtin_amdgcn_sched_barrier(0)
#define WAIT_VM(n) asm volatile("s_waitcnt vmcnt(" #n ")" ::: "memory")
#define BAR_LDS() do { asm volatile("s_waitcnt lgkmcnt(0)" ::: "memory"); __builtin_amdgcn_s_barrier(); asm volatile("" ::: "memory"); } while (0)

__device__ __forceinline__ f32x2 stat_partial(const u32x4& a, const u32x4& b) {
  float s = 0.f;
#pragma unroll
  for (int d = 0; d < 4; ++d) s += (bf_lo(a[d]) + bf_hi(a[d])) + (bf_lo(b[d]) + bf_hi(b[d]));
  s = red_add_g(s);
  const float m = s * (1.0f / 64.0f);
  float q = 0.f;
#pragma unroll
  for (int d = 0; d < 4; ++d) {
    float t;
    t = bf_lo(a[d]) - m; q = fmaf(t, t, q);
    t = bf_hi(a[d]) - m; q = fmaf(t, t, q);
    t = bf_lo(b[d]) - m; q = fmaf(t, t, q);
    t = bf_hi(b[d]) - m; q = fmaf(t, t, q);
  }
  q = red_add_g(q);
  return f32x2{s, q};
}
__device__ __forceinline__ f32x2 stat_combine(u32x4 raw, int g) {
  float s0 = __uint_as_float(raw[0]), q0 = __uint_as_float(raw[1]);
  float s1 = g < 3 ? __uint_as_float(raw[2]) : 0.f, q1 = g < 3 ? __uint_as_float(raw[3]) : 0.f;
  const float mean = red_add_g(s0 + s1) * (1.0f / D_);
  float d0 = s0 * (1.0f / 64.0f) - mean, d1 = s1 * (1.0f / 64.0f) - mean;
  float m2 = q0 + 64.0f * d0 * d0;
  if (g < 3) m2 += q1 + 64.0f * d1 * d1;
  m2 = red_add_g(m2);
  const float mb = rnd_bf16(mean);
  const float vb = rnd_bf16(m2 * (1.0f / D_));
  const float den = rnd_bf16(sqrtf(rnd_bf16(vb + 1e-5f)));
  return f32x2{mb, 1.0f / den};
}

__device__ __forceinline__ void tile_block(const uint16_t* __restrict__ src, int ld, int col0, const int (&rows)[8], char* dst,
                                           int jjstride, int lane, u32x4 (&o)[4]) {
  const int i = lane & 15;
  u32x2 v[8];
#pragma unroll
  for (int e = 0; e < 8; ++e) v[e] = *(const u32x2*)(src + (size_t)rows[e] * ld + col0 + 4 * i);
#pragma unroll
  for (int jj = 0; jj < 4; ++jj) {
#pragma unroll
    for (int d = 0; d < 4; ++d)
      o[jj][d] = __builtin_amdgcn_perm(v[2 * d + 1][jj >> 1], v[2 * d][jj >> 1], (jj & 1) ? 0x07060302u : 0x05040100u);
    *(u32x4*)(dst + (size_t)jj * jjstride + lane * 16) = o[jj];
  }
}

__global__ __launch_bounds__(512, 2) void k_prep(const uint16_t* __restrict__ x, const uint16_t* __restrict__ wqkv,
                                                 const uint16_t* __restrict__ wo, const uint16_t* __restrict__ wg,
                                                 const uint16_t* __restrict__ wu, const uint16_t* __restrict__ wd,
                                                 char* __restrict__ ws) {
  extern __shared__ __attribute__((aligned(16))) char smem[];
  const int tid = threadIdx.x, lane = tid & 63, wave = __builtin_amdgcn_readfirstlane(tid >> 6);
  const int b = blockIdx.x;
  const int g = lane >> 4;
  const u32x4 ones = {0x3f803f80u, 0x3f803f80u, 0x3f803f80u, 0x3f803f80u};
  if (b < 56) {
    const int which = b & 1, blk = b >> 1;
    f32x4 cs[4] = {f32x4{0, 0, 0, 0}, f32x4{0, 0, 0, 0}, f32x4{0, 0, 0, 0}, f32x4{0, 0, 0, 0}};
    if (wave < 7) {
#pragma unroll
      for (int it = 0; it < 2; ++it) {
        const int s = wave * 2 + it;
        int rows[8];
#pragma unroll
        for (int e = 0; e < 8; ++e) rows[e] = 32 * s + 8 * g + e;
        u32x4 o[4];
        tile_block(which ? wu : wg, F_, blk * 64, rows, ws + OFF_WGU + (size_t)(((blk * 4) * 14 + s) * 2 + which) * 1024,
                   14 * 2 * 1024, lane, o);
#pragma unroll
        for (int jj = 0; jj < 4; ++jj) cs[jj] = mfma16(o[jj], ones, cs[jj]);
      }
      if ((lane & 15) == 0) {
#pragma unroll
        for (int jj = 0; jj < 4; ++jj) *(f32x4*)(smem + ((wave * 4 + jj) * 4 + g) * 16) = cs[jj];
      }
    }
    __syncthreads();
    if (tid < 64) {
      float s = 0.f;
#pragma unroll
      for (int w = 0; w < 7; ++w) s += *(const float*)(smem + (w * 64 + tid) * 4);
      const int jj = tid >> 4, row = tid & 15;
      *(float*)(ws + OFF_CSGU + (size_t)(((blk * 4 + jj) * 2 + which) * 16 + row) * 4) = s;
    }
  } else if (b < 63) {
    const int nblk = b - 56;
    if (wave < 7) {
#pragma unroll
      for (int it = 0; it < 2; ++it) {
        const int s = wave * 2 + it;
        int rows[8];
#pragma unroll
        for (int e = 0; e < 8; ++e) rows[e] = 32 * s + 8 * g + e;
        u32x4 o[4];
        tile_block(wo, D_, nblk * 64, rows, ws + OFF_WO + (size_t)((nblk * 14 + s) * 4) * 1024, 1024, lane, o);
      }
    }
  } else if (b < 91) {
    const int t = b - 63;
    const int nblk = t >> 2, sq = t & 3;
    if (wave < 7) {
#pragma unroll
      for (int it = 0; it < 2; ++it) {
        const int s = sq * 14 + wave * 2 + it;
        int rows[8];
#pragma unroll
        for (int e = 0; e < 8; ++e) {
          int Fi = 2 * s + (e >> 2);
          rows[e] = 64 * (Fi >> 2) + 16 * g + 4 * (e & 3) + (Fi & 3);
        }
        u32x4 o[4];
        tile_block(wd, D_, nblk * 64, rows, ws + OFF_WD + (size_t)((nblk * 56 + s) * 4) * 1024, 1024, lane, o);
      }
    }
  } else if (b < 107) {
    const int head = b - 91;
    u32x2 v[21];
#pragma unroll
    for (int it = 0; it < 21; ++it) {
      const int c = it * 512 + tid;
      const int row = c / 24, grp = c - row * 24;
      const int part = grp >> 3, gi = grp & 7;
      v[it] = u32x2{0, 0};
      if (gi < 7) v[it] = *(const u32x2*)(wqkv + (size_t)row * (3 * D_) + part * D_ + head * DH + gi * 4);
    }
#pragma unroll
    for (int it = 0; it < 21; ++it) {
      const int c = it * 512 + tid;
      const int row = c / 24, grp = c - row * 24;
      int pp = (grp >> 2) + ((row >> 3) & 1);
      if (pp >= 6) pp -= 6;
      *(u32x2*)(smem + row * 192 + pp * 32 + (grp & 3) * 8) = v[it];
    }
    __syncthreads();
    if (wave < 6) {
      const int c = wave;
      const int q4 = (lane & 15) >> 2, p4 = lane & 3;
      int pp = c + (g & 1);
      if (pp >= 6) pp -= 6;
      const char* base = smem + (8 * g + q4) * 192 + pp * 32 + p4 * 8;
      f32x4 cs = {0, 0, 0, 0};
#pragma unroll
      for (int s = 0; s < KS; ++s) {
        u32x4 wf = tr_frag(base + s * 32 * 192, base + s * 32 * 192 + 4 * 192);
        *(u32x4*)(ws + OFF_W1 + (size_t)((head * 14 + s) * 6 + c) * 1024 + lane * 16) = wf;
        cs = mfma16(wf, ones, cs);
      }
      if ((lane & 15) == 0) *(f32x4*)(ws + OFF_CS1 + (size_t)(head * 96 + c * 16 + 4 * g) * 4) = cs;
    }
  } else if (b < 123) {
    const int tf = (b - 107) * 8 + wave, tok = lane & 15;
    u32x4 a[7][2];
#pragma unroll
    for (int nb = 0; nb < 7; ++nb)
#pragma unroll
      for (int h = 0; h < 2; ++h) a[nb][h] = *(const u32x4*)(x + (size_t)(tf * 16 + tok) * D_ + nb * 64 + 16 * g + 8 * h);
#pragma unroll
    for (int nb = 0; nb < 7; ++nb) {
#pragma unroll
      for (int h = 0; h < 2; ++h)
        *(u32x4*)(ws + OFF_X + (size_t)((tf * KS + 2 * nb + (g >> 1)) * 64 + (2 * (g & 1) + h) * 16 + tok) * 16) = a[nb][h];
      f32x2 st = stat_partial(a[nb][0], a[nb][1]);
      if (g == 0) *(f32x2*)(ws + OFF_STX + (size_t)((tf * 16 + tok) * 8 + nb) * 8) = st;
    }
  } else {
    for (unsigned o = tid * 16; o < FLAG_BYTES; o += 512 * 16) *(u32x4*)(ws + o) = u32x4{0, 0, 0, 0};
  }
}

static constexpr int LDS_KV = 114688;
static constexpr int LDS_TOTAL = 163840;

struct Ctx {
  __amdgpu_buffer_rsrc_t rs;
  char* ws;
};
__device__ __forceinline__ u32x4 ld_sc1(const Ctx& c, unsigned voff, unsigned soff) { return __builtin_amdgcn_raw_buffer_load_b128(c.rs, voff, soff, 16); }
__device__ __forceinline__ u32x4 ld_w(const Ctx& c, unsigned voff, unsigned soff) { return __builtin_amdgcn_raw_buffer_load_b128(c.rs, voff, soff, 0); }
__device__ __forceinline__ void st_sc1(const Ctx& c, unsigned voff, unsigned soff, u32x4 v) { __builtin_amdgcn_raw_buffer_store_b128(v, c.rs, voff, soff, 16); }
__device__ __forceinline__ void st_sc1_b64(const Ctx& c, unsigned voff, unsigned soff, u32x2 v) { __builtin_amdgcn_raw_buffer_store_b64(v, c.rs, voff, soff, 16); }
template <int AUX>
__device__ __forceinline__ void dma(const Ctx& c, unsigned voff, unsigned soff, char* lds_uniform) {
  __builtin_amdgcn_raw_ptr_buffer_load_lds(c.rs, (LAS void*)lds_uniform, 16, voff, soff, 0, AUX);
}
__device__ __forceinline__ void signal_add(const Ctx& c, unsigned off) {
  __hip_atomic_fetch_add((gu32*)(c.ws + off), 1u, __ATOMIC_RELAXED, __HIP_MEMORY_SCOPE_AGENT);
}
__device__ __forceinline__ void poll_ge(const Ctx& c, unsigned off, unsigned target) {
  gu32* f = (gu32*)(c.ws + off);
  gu32* tmo = (gu32*)(c.ws + OFF_TMO);
  unsigned spins = 0;
  while (__hip_atomic_load(f, __ATOMIC_RELAXED, __HIP_MEMORY_SCOPE_AGENT) < target) {
    ++spins;
    if ((spins & 1023u) == 0u) {
      if (spins > (1u << 20) || __hip_atomic_load(tmo, __ATOMIC_RELAXED, __HIP_MEMORY_SCOPE_AGENT) != 0u) {
        __hip_atomic_store(tmo, 1u, __ATOMIC_RELAXED, __HIP_MEMORY_SCOPE_AGENT);
        break;
      }
    }
    __builtin_amdgcn_s_sleep(1);
  }
  __builtin_amdgcn_fence(__ATOMIC_ACQUIRE, "wavefront");
}
__device__ __forceinline__ unsigned cnt_off(int L, int ph, int p) { return OFF_CNT + (unsigned)(((L * 4 + ph) * 16 + p) * 256); }
__device__ __forceinline__ unsigned kvf_off(int L, int p, int h) { return OFF_KVF + (unsigned)(((L * 16 + p) * 16 + h) * 128); }

template <bool MASKALL>
__device__ __forceinline__ void attn_body(const Ctx& cx, const char* kv, u32x4 qf, int w, int lane, int p, unsigned ooff, int head) {
  const int tok = lane & 15, g = lane >> 4;
  const int t0 = w & ~1;
  const char* kl = kv + t0 * 1024 + lane * 16;
  f32x4 sc[18];
#pragma unroll
  for (int i = 0; i < 18; ++i) {
    u32x4 kf = *(const u32x4*)(kl + i * 1024);
    sc[i] = mfma16(kf, qf, f32x4{0, 0, 0, 0});
  }
  const int dq = 16 * (16 + w) + tok - (16 * t0 + 4 * g);
  const int kneg = 16 * (16 - 8 * p);
  const float NEG = -INFINITY;
#pragma unroll
  for (int i = 0; i < 18; ++i) {
    if (MASKALL || i < 2 || i >= 16) {
#pragma unroll
      for (int r = 0; r < 4; ++r) {
        int d = dq - 16 * i - r;
        bool ok = (unsigned)d < 256u;
        if (MASKALL) ok = ok && (16 * (t0 + i) + 4 * g + r >= kneg);
        sc[i][r] = ok ? sc[i][r] : NEG;
      }
    }
  }
  float m = NEG;
#pragma unroll
  for (int i = 0; i < 18; ++i) m = fmaxf(m, fmaxf(fmaxf(sc[i][0], sc[i][1]), fmaxf(sc[i][2], sc[i][3])));
  m = red_max_g(m);
  const float cs = 0.18898223650461362f * 1.4426950408889634f;
  const float mc = m * cs;
  u32x4 pf[9];
#pragma unroll
  for (int i = 0; i < 18; ++i) {
    float e0 = __builtin_amdgcn_exp2f(fmaf(sc[i][0], cs, -mc));
    float e1 = __builtin_amdgcn_exp2f(fmaf(sc[i][1], cs, -mc));
    float e2 = __builtin_amdgcn_exp2f(fmaf(sc[i][2], cs, -mc));
    float e3 = __builtin_amdgcn_exp2f(fmaf(sc[i][3], cs, -mc));
    pf[i >> 1][(i & 1) * 2 + 0] = pk_bf16(e0, e1);
    pf[i >> 1][(i & 1) * 2 + 1] = pk_bf16(e2, e3);
  }
  const int q4 = (lane & 15) >> 2, p4 = lane & 3;
  const char* vl = kv + 24 * 1024 + t0 * 1024;
  const int vb = p4 * 256 + (((4 * g + q4) * 16 + (p4 >> 1) * 128) & 255);
  const int vofs[2] = {vb + (p4 & 1) * 8, vb + ((p4 & 1) ^ 1) * 8};
  f32x4 o[2] = {f32x4{0, 0, 0, 0}, f32x4{0, 0, 0, 0}};
#pragma unroll
  for (int bb = 0; bb < 9; ++bb) {
#pragma unroll
    for (int f = 0; f < 2; ++f) {
      u32x4 vf = tr_frag(vl + vofs[f] + (2 * bb) * 1024, vl + vofs[f] + (2 * bb + 1) * 1024);
      o[f] = mfma16(vf, pf[bb], o[f]);
    }
  }
  const float lsum = __shfl(o[1][0], 48 + tok);
  const float inv = 1.0f / lsum;
#pragma unroll
  for (int f = 0; f < 2; ++f) {
    if (f == 0 || g < 3) {
      const int k0 = DH * head + 16 * f + 4 * g;
      u32x2 st = {pk_bf16(o[f][0] * inv, o[f][1] * inv), pk_bf16(o[f][2] * inv, o[f][3] * inv)};
      st_sc1_b64(cx, (unsigned)(((k0 >> 5) * 64 + ((k0 & 31) >> 3) * 16 + tok) * 16 + (k0 & 7) * 2), ooff, st);
    }
  }
}

__device__ __forceinline__ void proj_finish(const f32x4 (&fin)[4], const u32x4 (&rs)[2], u32x4 (&o)[2]) {
#pragma unroll
  for (int h = 0; h < 2; ++h)
#pragma unroll
    for (int d = 0; d < 4; ++d) {
      const int c0 = 8 * h + 2 * d;
      unsigned y = pk_bf16(fin[c0 & 3][c0 >> 2], fin[(c0 + 1) & 3][(c0 + 1) >> 2]);
      o[h][d] = pk_bf16(bf_lo(rs[h][d]) + bf_lo(y), bf_hi(rs[h][d]) + bf_hi(y));
    }
}

__device__ __forceinline__ void p3_compute(char* smem, const u32x4 (&a3)[7], const u32x4 (&rs)[2], int wave, int lane) {
  const int t3 = wave & 3, kh = wave >> 2, tok = lane & 15, g = lane >> 4;
  const unsigned l16 = lane * 16;
  f32x4 acc[4];
#pragma unroll
  for (int jj = 0; jj < 4; ++jj) acc[jj] = f32x4{0, 0, 0, 0};
  const char* wl = smem + kh * 28 * 1024 + l16;
#pragma unroll
  for (int i = 0; i < 7; ++i)
#pragma unroll
    for (int jj = 0; jj < 4; ++jj) {
      u32x4 wf = *(const u32x4*)(wl + (i * 4 + jj) * 1024);
      acc[jj] = mfma16(wf, a3[i], acc[jj]);
    }
  if (kh == 1) {
#pragma unroll
    for (int jj = 0; jj < 4; ++jj) *(f32x4*)(smem + 65536 + (t3 * 4 + jj) * 1024 + l16) = acc[jj];
  }
  BAR_LDS();
  if (kh == 0) {
#pragma unroll
    for (int jj = 0; jj < 4; ++jj) acc[jj] += *(const f32x4*)(smem + 65536 + (t3 * 4 + jj) * 1024 + l16);
    u32x4 o[2];
    proj_finish(acc, rs, o);
    f32x2 st = stat_partial(o[0], o[1]);
    const int so = t3 * 2048 + (g >> 1) * 1024 + ((2 * (g & 1)) * 16 + tok) * 16;
    *(u32x4*)(smem + LDS_KV + so) = o[0];
    *(u32x4*)(smem + LDS_KV + so + 256) = o[1];
    if (g == 0) *(f32x2*)(smem + LDS_KV + 8192 + (t3 * 16 + tok) * 8) = st;
  }
  BAR_LDS();
}

__global__ __launch_bounds__(512, 2) void k_main(char* __restrict__ ws_, char* __restrict__ out) {
  extern __shared__ __attribute__((aligned(16))) char smem[];
  Ctx cx;
  cx.ws = ws_;
  cx.rs = __builtin_amdgcn_make_buffer_rsrc(ws_, 0, 0x7fffffff, 0x00020000);
  {
    const int wave0 = __builtin_amdgcn_readfirstlane(threadIdx.x >> 6), j0 = (blockIdx.x >> 3) & 15;
#pragma unroll
    for (int i = 0; i < 11; ++i) {
      const int idx = wave0 * 11 + i;
      if (idx < 84) dma<0>(cx, (threadIdx.x & 63) * 16, OFF_W1 + (unsigned)(j0 * 84 + idx) * 1024, smem + idx * 1024);
    }
  }

#pragma unroll 1
  for (int L = 0; L < 2; ++L) {
    int tid = threadIdx.x, b = blockIdx.x;
    asm volatile("" : "+v"(tid));
    asm volatile("" : "+s"(b));
    const int lane = tid & 63, wave = __builtin_amdgcn_readfirstlane(tid >> 6);
    const int p = (b & 7) * 2 + (b >> 7), j = (b >> 3) & 15;
    const int tok = lane & 15, g = lane >> 4;
    const bool projwg = j < 14;
    const int hh = j >= 7 ? 1 : 0, nblk = projwg ? j - 7 * hh : 6;
    const unsigned l16 = lane * 16;
    const unsigned xlane = (unsigned)((g >> 1) * 1024 + ((2 * (g & 1)) * 16 + tok) * 16);
    const unsigned stlane = (unsigned)(tok * 64 + g * 16);
    const int tfw = p * 8 + wave;
    if (L > 0) {
      if (tid == 448) poll_ge(cx, cnt_off(L - 1, 3, p), 14);
      __syncthreads();
    }
    u32x4 qf;
    {
      u32x4 a[KS];
#pragma unroll
      for (int s = 0; s < KS; ++s) a[s] = ld_sc1(cx, l16, OFF_X + L * XT_BYTES + (unsigned)(tfw * KS + s) * 1024);
      u32x4 straw = ld_sc1(cx, stlane, OFF_STX + L * ST_BYTES + (unsigned)(tfw * 1024));
      u32x4 csr[6];
#pragma unroll
      for (int c = 0; c < 6; ++c) csr[c] = ld_w(cx, g * 16, OFF_CS1 + (unsigned)(j * 96 + c * 16) * 4);
      SB();
      if (p < 2) {
#pragma unroll
        for (int i = 0; i < 2; ++i) {
          const int ti = wave * 2 + i;
          if (p * 8 - 16 + ti < 0) {
            *(u32x4*)(smem + LDS_KV + ti * 1024 + l16) = u32x4{0, 0, 0, 0};
            *(u32x4*)(smem + LDS_KV + (24 + ti) * 1024 + l16) = u32x4{0, 0, 0, 0};
          }
        }
      }
      WAIT_VM(21);
      __builtin_amdgcn_s_barrier();
      SB();
      f32x4 acc[6];
#pragma unroll
      for (int c = 0; c < 6; ++c) acc[c] = f32x4{0, 0, 0, 0};
      u32x4 wb[2][6];
#pragma unroll
      for (int c = 0; c < 6; ++c) wb[0][c] = *(const u32x4*)(smem + c * 1024 + l16);
#pragma unroll
      for (int s = 0; s < KS; ++s) {
        if (s + 1 < KS) {
#pragma unroll
          for (int c = 0; c < 6; ++c) wb[(s + 1) & 1][c] = *(const u32x4*)(smem + ((s + 1) * 6 + c) * 1024 + l16);
        }
#pragma unroll
        for (int c = 0; c < 6; ++c) acc[c] = mfma16(wb[s & 1][c], a[s], acc[c]);
        SB();
      }
      const f32x2 st = stat_combine(straw, g);
      unsigned pk[6][2];
#pragma unroll
      for (int c = 0; c < 6; ++c) {
        float v[4];
#pragma unroll
        for (int r = 0; r < 4; ++r) v[r] = (acc[c][r] - st[0] * __uint_as_float(csr[c][r])) * st[1];
        if (c == 5 && g == 3) v[0] = 1.0f;
        pk[c][0] = pk_bf16(v[0], v[1]);
        pk[c][1] = pk_bf16(v[2], v[3]);
      }
      qf = u32x4{pk[0][0], pk[0][1], pk[1][0], pk[1][1]};
      u32x4 kf = {pk[2][0], pk[2][1], pk[3][0], pk[3][1]};
      u32x4 vf = (g & 1) ? u32x4{pk[5][0], pk[5][1], pk[4][0], pk[4][1]} : u32x4{pk[4][0], pk[4][1], pk[5][0], pk[5][1]};
      const unsigned vslot = (unsigned)(g * 256 + ((tok * 16 + (g >> 1) * 128) & 255));
      st_sc1(cx, l16, OFF_K + L * KV_BYTES + (unsigned)(j * 128 + tfw) * 1024, kf);
      st_sc1(cx, vslot, OFF_V + L * KV_BYTES + (unsigned)(j * 128 + tfw) * 1024, vf);
      *(u32x4*)(smem + LDS_KV + (16 + wave) * 1024 + l16) = kf;
      *(u32x4*)(smem + LDS_KV + (24 + 16 + wave) * 1024 + vslot) = vf;
      WAIT_VM(0);
      __syncthreads();
    }
    if (tid == 448) {
      signal_add(cx, kvf_off(L, p, j));
      if (p >= 1) poll_ge(cx, kvf_off(L, p - 1, j), 1);
      if (p >= 2) poll_ge(cx, kvf_off(L, p - 2, j), 1);
    }
    if (projwg && wave < 7) {
#pragma unroll
      for (int i = 0; i < 8; ++i) dma<0>(cx, l16, OFF_WO + (unsigned)(nblk * 56 + wave * 8 + i) * 1024, smem + (wave * 8 + i) * 1024);
    }
    __builtin_amdgcn_s_barrier();
    SB();
#pragma unroll
    for (int i = 0; i < 2; ++i) {
      const int ti = wave * 2 + i, T = p * 8 - 16 + ti;
      if (T >= 0) {
        dma<16>(cx, l16, OFF_K + L * KV_BYTES + (unsigned)(j * 128 + T) * 1024, smem + LDS_KV + ti * 1024);
        dma<16>(cx, l16, OFF_V + L * KV_BYTES + (unsigned)(j * 128 + T) * 1024, smem + LDS_KV + (24 + ti) * 1024);
      }
    }
    WAIT_VM(0);
    __syncthreads();
    {
      const unsigned ooff = OFF_O + L * XT_BYTES + (unsigned)(tfw * KS) * 1024;
      if (p >= 2) attn_body<false>(cx, smem + LDS_KV, qf, wave, lane, p, ooff, j);
      else attn_body<true>(cx, smem + LDS_KV, qf, wave, lane, p, ooff, j);
    }
    WAIT_VM(0);
    __syncthreads();
    if (tid == 448) {
      signal_add(cx, cnt_off(L, 0, p));
      if (projwg) poll_ge(cx, cnt_off(L, 0, p), 16);
    }
    __syncthreads();
    const int tf3 = p * 8 + hh * 4 + (wave & 3);
    const unsigned xsoff = (unsigned)(tf3 * KS + 2 * nblk) * 1024;
    u32x4 a3[7], rs[2];
    {
      const unsigned abase = (projwg ? OFF_O : OFF_X) + L * XT_BYTES + (unsigned)(tf3 * KS + (wave >> 2) * 7) * 1024;
#pragma unroll
      for (int i = 0; i < 7; ++i) a3[i] = ld_sc1(cx, l16, abase + i * 1024);
      rs[0] = ld_sc1(cx, xlane, OFF_X + L * XT_BYTES + xsoff);
      rs[1] = ld_sc1(cx, xlane + 256, OFF_X + L * XT_BYTES + xsoff);
    }
    SB();
    if (wave < 7) {
      const int Fi = j * 7 + wave;
      u32x4 w4[KS][2];
#pragma unroll
      for (int s = 0; s < KS; ++s) {
        w4[s][0] = ld_w(cx, l16, OFF_WGU + (unsigned)((Fi * KS + s) * 2) * 1024);
        w4[s][1] = ld_w(cx, l16, OFF_WGU + (unsigned)((Fi * KS + s) * 2 + 1) * 1024);
      }
      SB();
      if (projwg) p3_compute(smem, a3, rs, wave, lane);
      __builtin_amdgcn_s_barrier();
      SB();
#pragma unroll
      for (int s = 0; s < KS; ++s) dma<16>(cx, l16, OFF_X1 + L * XT_BYTES + (unsigned)(tfw * KS + s) * 1024, smem + (wave * KS + s) * 1024);
      u32x4 straw = ld_sc1(cx, stlane, OFF_STX1 + L * ST_BYTES + (unsigned)(tfw * 1024));
      u32x4 cg0 = ld_w(cx, g * 16, OFF_CSGU + (unsigned)(Fi * 2 + 0) * 64);
      u32x4 cu0 = ld_w(cx, g * 16, OFF_CSGU + (unsigned)(Fi * 2 + 1) * 64);
      {
        const f32x2 st = stat_combine(straw, g);
        if (g == 0) *(f32x2*)(smem + LDS_KV + 32768 + (wave * 16 + tok) * 8) = st;
        if (tok == 0) {
          *(u32x4*)(smem + LDS_KV + 34816 + wave * 128 + g * 16) = cg0;
          *(u32x4*)(smem + LDS_KV + 34816 + wave * 128 + 64 + g * 16) = cu0;
        }
      }
      WAIT_VM(0);
      __syncthreads();
      u32x4 w5[8][4];
      {
        f32x4 acc[8][2];
#pragma unroll
        for (int t = 0; t < 8; ++t) { acc[t][0] = f32x4{0, 0, 0, 0}; acc[t][1] = f32x4{0, 0, 0, 0}; }
        u32x4 xb[3][4];
        auto rd = [&](int hs) {
#pragma unroll
          for (int t = 0; t < 4; ++t) xb[hs % 3][t] = *(const u32x4*)(smem + (((hs & 1) * 4 + t) * KS + (hs >> 1)) * 1024 + l16);
        };
        rd(0); rd(1);
        SB();
        const unsigned w5base = OFF_WD + (unsigned)((nblk * 56 + wave * 8) * 4) * 1024;
#pragma unroll
        for (int hs = 0; hs < 28; ++hs) {
          const int s = hs >> 1, th = hs & 1;
          if (hs + 2 < 28) rd(hs + 2);
#pragma unroll
          for (int t = 0; t < 4; ++t) {
            acc[th * 4 + t][0] = mfma16(w4[s][0], xb[hs % 3][t], acc[th * 4 + t][0]);
            acc[th * 4 + t][1] = mfma16(w4[s][1], xb[hs % 3][t], acc[th * 4 + t][1]);
          }
          SB();
          if (th == 1) {
#pragma unroll
            for (int q = 0; q < 2; ++q) {
              const int idx = s * 2 + q;
              w5[idx >> 2][idx & 3] = ld_w(cx, l16, w5base + idx * 1024);
            }
            SB();
          }
        }
#pragma unroll
        for (int idx = 28; idx < 32; ++idx) w5[idx >> 2][idx & 3] = ld_w(cx, l16, w5base + idx * 1024);
        SB();
        const u32x4 cg = *(const u32x4*)(smem + LDS_KV + 34816 + wave * 128 + g * 16);
        const u32x4 cu = *(const u32x4*)(smem + LDS_KV + 34816 + wave * 128 + 64 + g * 16);
#pragma unroll
        for (int t = 0; t < 8; ++t) {
          const f32x2 s2 = *(const f32x2*)(smem + LDS_KV + 32768 + (t * 16 + tok) * 8);
          float gv[4], uv[4];
#pragma unroll
          for (int r = 0; r < 4; ++r) {
            gv[r] = (acc[t][0][r] - s2[0] * __uint_as_float(cg[r])) * s2[1];
            uv[r] = (acc[t][1][r] - s2[0] * __uint_as_float(cu[r])) * s2[1];
          }
          unsigned g0 = pk_bf16(gv[0], gv[1]), g1 = pk_bf16(gv[2], gv[3]);
          unsigned u0 = pk_bf16(uv[0], uv[1]), u1 = pk_bf16(uv[2], uv[3]);
          u32x2 o = {pk_bf16(bf_lo(g0) * bf_lo(u0), bf_hi(g0) * bf_hi(u0)), pk_bf16(bf_lo(g1) * bf_lo(u1), bf_hi(g1) * bf_hi(u1))};
          *(u32x2*)(smem + LDS_KV + (t * 7 + wave) * 512 + lane * 8) = o;
        }
      }
      asm volatile("s_waitcnt lgkmcnt(0)" ::: "memory");
      __builtin_amdgcn_s_barrier();
      __builtin_amdgcn_s_barrier();
      SB();
      if (projwg) {
        const int tfb = p * 8 + hh * 4;
        f32x4 acc[4][4];
#pragma unroll
        for (int jj = 0; jj < 4; ++jj)
#pragma unroll
          for (int t = 0; t < 4; ++t) acc[jj][t] = f32x4{0, 0, 0, 0};
        u32x4 hb[3][4];
        const unsigned hbase = OFF_H + L * H_BYTES + (unsigned)(tfb * KSF + wave * 8) * 1024;
        auto ldh = [&](int i) {
#pragma unroll
          for (int t = 0; t < 4; ++t) hb[i % 3][t] = ld_sc1(cx, l16, hbase + (unsigned)(t * KSF + i) * 1024);
        };
        ldh(0); ldh(1); ldh(2);
        SB();
#pragma unroll
        for (int i = 0; i < 8; ++i) {
#pragma unroll
          for (int jj = 0; jj < 4; ++jj)
#pragma unroll
            for (int t = 0; t < 4; ++t) acc[jj][t] = mfma16(w5[i][jj], hb[i % 3][t], acc[jj][t]);
          SB();
          if (i + 3 < 8) ldh(i + 3);
          SB();
        }
        u32x4 r5[2];
        r5[0] = ld_sc1(cx, xlane, OFF_X1 + L * XT_BYTES + xsoff);
        r5[1] = ld_sc1(cx, xlane + 256, OFF_X1 + L * XT_BYTES + xsoff);
#pragma unroll
        for (int t = 0; t < 4; ++t) {
          if (t != wave) {
#pragma unroll
            for (int jj = 0; jj < 4; ++jj) *(f32x4*)(smem + ((wave * 4 + t) * 4 + jj) * 1024 + l16) = acc[jj][t];
          }
        }
        asm volatile("s_waitcnt lgkmcnt(0)" ::: "memory");
        __builtin_amdgcn_s_barrier();
        SB();
        if (wave < 4) {
          f32x4 fin[4];
#pragma unroll
          for (int jj = 0; jj < 4; ++jj) fin[jj] = wave == 0 ? acc[jj][0] : wave == 1 ? acc[jj][1] : wave == 2 ? acc[jj][2] : acc[jj][3];
#pragma unroll
          for (int sw = 0; sw < 7; ++sw) {
            if (sw != wave) {
#pragma unroll
              for (int jj = 0; jj < 4; ++jj) fin[jj] += *(const f32x4*)(smem + ((sw * 4 + wave) * 4 + jj) * 1024 + l16);
            }
          }
          u32x4 o[2];
          proj_finish(fin, r5, o);
          if (L == 0) {
            f32x2 st = stat_partial(o[0], o[1]);
            st_sc1(cx, xlane, OFF_X + XT_BYTES + xsoff, o[0]);
            st_sc1(cx, xlane + 256, OFF_X + XT_BYTES + xsoff, o[1]);
            if (g == 0) st_sc1_b64(cx, (unsigned)(tok * 64), OFF_STX + ST_BYTES + (unsigned)(tf3 * 1024 + nblk * 8), __builtin_bit_cast(u32x2, st));
          } else {
            char* op = out + ((size_t)(tf3 * 16 + tok) * D_ + nblk * 64 + 16 * g) * 2;
            *(u32x4*)op = o[0];
            *(u32x4*)(op + 16) = o[1];
          }
        }
      }
      WAIT_VM(0);
    } else {
      if (projwg) {
        p3_compute(smem, a3, rs, wave, lane);
#pragma unroll
        for (int i = 0; i < 8; ++i) {
          u32x4 v = *(const u32x4*)(smem + LDS_KV + i * 1024 + l16);
          st_sc1(cx, l16, OFF_X1 + L * XT_BYTES + (unsigned)(((p * 8 + hh * 4 + (i >> 1)) * KS + 2 * nblk + (i & 1)) * 1024), v);
        }
        u32x2 sv = *(const u32x2*)(smem + LDS_KV + 8192 + lane * 8);
        st_sc1_b64(cx, (unsigned)(lane * 64), OFF_STX1 + L * ST_BYTES + (unsigned)((p * 128 + hh * 64) * 64 + nblk * 8), sv);
        WAIT_VM(0);
        if (lane == 0) signal_add(cx, cnt_off(L, 1, p));
      }
      if (lane == 0) poll_ge(cx, cnt_off(L, 1, p), 14);
      __builtin_amdgcn_s_barrier();
      SB();
#pragma unroll
      for (int s = 0; s < KS; ++s) dma<16>(cx, l16, OFF_X1 + L * XT_BYTES + (unsigned)(tfw * KS + s) * 1024, smem + (wave * KS + s) * 1024);
      u32x4 straw = ld_sc1(cx, stlane, OFF_STX1 + L * ST_BYTES + (unsigned)(tfw * 1024));
      {
        const f32x2 st = stat_combine(straw, g);
        if (g == 0) *(f32x2*)(smem + LDS_KV + 32768 + (wave * 16 + tok) * 8) = st;
      }
      WAIT_VM(0);
      __syncthreads();
      __builtin_amdgcn_s_barrier();
      SB();
#pragma unroll
      for (int t = 0; t < 8; ++t)
#pragma unroll
        for (int w = 0; w < 7; ++w) {
          u32x2 v = *(const u32x2*)(smem + LDS_KV + (t * 7 + w) * 512 + lane * 8);
          const int Fw = j * 7 + w;
          st_sc1_b64(cx, l16, OFF_H + L * H_BYTES + (unsigned)(((p * 8 + t) * KSF + (Fw >> 1)) * 1024 + (Fw & 1) * 8), v);
        }
      WAIT_VM(0);
      if (lane == 0) {
        signal_add(cx, cnt_off(L, 2, p));
        if (projwg) poll_ge(cx, cnt_off(L, 2, p), 16);
      }
      __builtin_amdgcn_s_barrier();
      if (projwg) __builtin_amdgcn_s_barrier();
    }
    if (L == 0) {
      __syncthreads();
      if (tid == 448 && projwg) signal_add(cx, cnt_off(L, 3, p));
      SB();
#pragma unroll
      for (int i = 0; i < 11; ++i) {
        const int idx = wave * 11 + i;
        if (idx < 84) dma<0>(cx, l16, OFF_W1 + (unsigned)(j * 84 + idx) * 1024, smem + idx * 1024);
      }
    }
  }
}

extern "C" void kernel_launch(void* const* d_in, const int* in_sizes, int n_in, void* d_out, int out_size, void* d_ws,
                              size_t ws_size, hipStream_t stream) {
  const uint16_t* x = (const uint16_t*)d_in[0];
  const uint16_t* wqkv = (const uint16_t*)d_in[1];
  const uint16_t* wo = (const uint16_t*)d_in[2];
  const uint16_t* wg = (const uint16_t*)d_in[4];
  const uint16_t* wu = (const uint16_t*)d_in[5];
  const uint16_t* wd = (const uint16_t*)d_in[6];
  char* ws = (char*)d_ws;
  constexpr int LP = 448 * 192;
  (void)hipFuncSetAttribute((const void*)k_prep, hipFuncAttributeMaxDynamicSharedMemorySize, LP);
  (void)hipFuncSetAttribute((const void*)k_main, hipFuncAttributeMaxDynamicSharedMemorySize, LDS_TOTAL);
  k_prep<<<124, 512, LP, stream>>>(x, wqkv, wo, wg, wu, wd, ws);
  k_main<<<256, 512, LDS_TOTAL, stream>>>(ws, (char*)d_out);
}
```

```cpp
#include <hip/hip_runtime.h>
#include <hip/hip_bf16.h>
#include <stdint.h>

typedef __attribute__((ext_vector_type(8))) short s16x8;
typedef __attribute__((ext_vector_type(4))) short s16x4;
typedef __attribute__((ext_vector_type(4))) float f32x4;
typedef __attribute__((ext_vector_type(2))) float f32x2;
typedef __attribute__((ext_vector_type(2))) __bf16 bf16x2;
typedef __attribute__((ext_vector_type(4))) unsigned u32x4;
typedef __attribute__((ext_vector_type(2))) unsigned u32x2;
#define LAS __attribute__((address_space(3)))
typedef __attribute__((address_space(1))) unsigned gu32;

static constexpr int S_ = 2048, D_ = 448, F_ = 1792, NH = 16, DH = 28;
static constexpr int KS = 14, KSF = 56;

static constexpr unsigned OFF_CNT = 0;
static constexpr unsigned OFF_KVF = 32768;
static constexpr unsigned OFF_TMO = 98304;
static constexpr unsigned FLAG_BYTES = 98304 + 1024;
static constexpr unsigned OFF_W1 = 131072;
static constexpr unsigned OFF_CS1 = OFF_W1 + 16 * 14 * 6 * 1024;
static constexpr unsigned OFF_WO = OFF_CS1 + 16384;
static constexpr unsigned OFF_WGU = OFF_WO + 7 * 14 * 4 * 1024;
static constexpr unsigned OFF_CSGU = OFF_WGU + 112 * 14 * 2 * 1024;
static constexpr unsigned OFF_WD = OFF_CSGU + 32768;
static constexpr unsigned XT_BYTES = 128 * 14 * 1024;
static constexpr unsigned OFF_X = OFF_WD + 7 * 56 * 4 * 1024;
static constexpr unsigned OFF_X1 = OFF_X + 2 * XT_BYTES;
static constexpr unsigned OFF_O = OFF_X1 + 2 * XT_BYTES;
static constexpr unsigned KV_BYTES = 16 * 128 * 1024;
static constexpr unsigned OFF_K = OFF_O + 2 * XT_BYTES;
static constexpr unsigned OFF_V = OFF_K + 2 * KV_BYTES;
static constexpr unsigned H_BYTES = 128 * 56 * 1024;
static constexpr unsigned OFF_H = OFF_V + 2 * KV_BYTES;
static constexpr unsigned ST_BYTES = 2048 * 64;
static constexpr unsigned OFF_STX = OFF_H + 2 * H_BYTES;
static constexpr unsigned OFF_STX1 = OFF_STX + 2 * ST_BYTES;

__device__ __forceinline__ float bf_lo(unsigned u) { return __uint_as_float(u << 16); }
__device__ __forceinline__ float bf_hi(unsigned u) { return __uint_as_float(u & 0xffff0000u); }
__device__ __forceinline__ unsigned pk_bf16(float a, float b) {
  f32x2 f = {a, b};
  bf16x2 r = __builtin_convertvector(f, bf16x2);
  return __builtin_bit_cast(unsigned, r);
}
__device__ __forceinline__ float rnd_bf16(float a) { return bf_lo(pk_bf16(a, 0.f)); }
__device__ __forceinline__ float red_add_g(float v) {
  auto p = __builtin_amdgcn_permlane16_swap(__float_as_uint(v), __float_as_uint(v), false, false);
  v = __uint_as_float(p[0]) + __uint_as_float(p[1]);
  auto q = __builtin_amdgcn_permlane32_swap(__float_as_uint(v), __float_as_uint(v), false, false);
  return __uint_as_float(q[0]) + __uint_as_float(q[1]);
}
__device__ __forceinline__ float red_max_g(float v) {
  auto p = __builtin_amdgcn_permlane16_swap(__float_as_uint(v), __float_as_uint(v), false, false);
  v = fmaxf(__uint_as_float(p[0]), __uint_as_float(p[1]));
  auto q = __builtin_amdgcn_permlane32_swap(__float_as_uint(v), __float_as_uint(v), false, false);
  return fmaxf(__uint_as_float(q[0]), __uint_as_float(q[1]));
}
__device__ __forceinline__ f32x4 mfma16(u32x4 a, u32x4 b, f32x4 c) {
  return __builtin_amdgcn_mfma_f32_16x16x32_bf16(__builtin_bit_cast(s16x8, a), __builtin_bit_cast(s16x8, b), c, 0, 0, 0);
}
__device__ __forceinline__ u32x4 tr_frag(const char* p0, const char* p1) {
  s16x4 lo = __builtin_amdgcn_ds_read_tr16_b64_v4i16((LAS s16x4*)p0);
  s16x4 hi = __builtin_amdgcn_ds_read_tr16_b64_v4i16((LAS s16x4*)p1);
  u32x2 l2 = __builtin_bit_cast(u32x2, lo), h2 = __builtin_bit_cast(u32x2, hi);
  return u32x4{l2[0], l2[1], h2[0], h2[1]};
}
#define SB() __builtin_amdgcn_sched_barrier(0)
#define STAMP(i)
#define WAIT_VM(n) asm volatile("s_waitcnt vmcnt(" #n ")" ::: "memory")
#define BAR_LDS() do { asm volatile("s_waitcnt lgkmcnt(0)" ::: "memory"); __builtin_amdgcn_s_barrier(); asm volatile("" ::: "memory"); } while (0)

__device__ __forceinline__ f32x2 stat_partial(const u32x4& a, const u32x4& b) {
  float s = 0.f;
#pragma unroll
  for (int d = 0; d < 4; ++d) s += (bf_lo(a[d]) + bf_hi(a[d])) + (bf_lo(b[d]) + bf_hi(b[d]));
  s = red_add_g(s);
  const float m = s * (1.0f / 64.0f);
  float q = 0.f;
#pragma unroll
  for (int d = 0; d < 4; ++d) {
    float t;
    t = bf_lo(a[d]) - m; q = fmaf(t, t, q);
    t = bf_hi(a[d]) - m; q = fmaf(t, t, q);
    t = bf_lo(b[d]) - m; q = fmaf(t, t, q);
    t = bf_hi(b[d]) - m; q = fmaf(t, t, q);
  }
  q = red_add_g(q);
  return f32x2{s, q};
}
__device__ __forceinline__ f32x2 stat_combine(u32x4 raw, int g) {
  float s0 = __uint_as_float(raw[0]), q0 = __uint_as_float(raw[1]);
  float s1 = g < 3 ? __uint_as_float(raw[2]) : 0.f, q1 = g < 3 ? __uint_as_float(raw[3]) : 0.f;
  const float mean = red_add_g(s0 + s1) * (1.0f / D_);
  float d0 = s0 * (1.0f / 64.0f) - mean, d1 = s1 * (1.0f / 64.0f) - mean;
  float m2 = q0 + 64.0f * d0 * d0;
  if (g < 3) m2 += q1 + 64.0f * d1 * d1;
  m2 = red_add_g(m2);
  const float mb = rnd_bf16(mean);
  const float vb = rnd_bf16(m2 * (1.0f / D_));
  const float den = rnd_bf16(sqrtf(rnd_bf16(vb + 1e-5f)));
  return f32x2{mb, 1.0f / den};
}

__device__ __forceinline__ void tile_block(const uint16_t* __restrict__ src, int ld, int col0, const int (&rows)[8], char* dst,
                                           int jjstride, int lane, u32x4 (&o)[4]) {
  const int i = lane & 15;
  u32x2 v[8];
#pragma unroll
  for (int e = 0; e < 8; ++e) v[e] = *(const u32x2*)(src + (size_t)rows[e] * ld + col0 + 4 * i);
#pragma unroll
  for (int jj = 0; jj < 4; ++jj) {
#pragma unroll
    for (int d = 0; d < 4; ++d)
      o[jj][d] = __builtin_amdgcn_perm(v[2 * d + 1][jj >> 1], v[2 * d][jj >> 1], (jj & 1) ? 0x07060302u : 0x05040100u);
    *(u32x4*)(dst + (size_t)jj * jjstride + lane * 16) = o[jj];
  }
}

__global__ __launch_bounds__(512, 2) void k_prep(const uint16_t* __restrict__ x, const uint16_t* __restrict__ wqkv,
                                                 const uint16_t* __restrict__ wo, const uint16_t* __restrict__ wg,
                                                 const uint16_t* __restrict__ wu, const uint16_t* __restrict__ wd,
                                                 char* __restrict__ ws) {
  extern __shared__ __attribute__((aligned(16))) char smem[];
  const int tid = threadIdx.x, lane = tid & 63, wave = __builtin_amdgcn_readfirstlane(tid >> 6);
  const int b = blockIdx.x;
  const int g = lane >> 4;
  const u32x4 ones = {0x3f803f80u, 0x3f803f80u, 0x3f803f80u, 0x3f803f80u};
  if (b < 112) {
    const int kh = b & 1, which = (b >> 1) & 1, blk = b >> 2;
    if (wave < 7) {
      const int s = kh * 7 + wave;
      int rows[8];
#pragma unroll
      for (int e = 0; e < 8; ++e) rows[e] = 32 * s + 8 * g + e;
      u32x4 o[4];
      tile_block(which ? wu : wg, F_, blk * 64, rows, ws + OFF_WGU + (size_t)(((blk * 4) * 14 + s) * 2 + which) * 1024,
                 14 * 2 * 1024, lane, o);
#pragma unroll
      for (int jj = 0; jj < 4; ++jj) {
        f32x4 cs = mfma16(o[jj], ones, f32x4{0, 0, 0, 0});
        if ((lane & 15) == 0) *(f32x4*)(smem + ((wave * 4 + jj) * 4 + g) * 16) = cs;
      }
    }
    __syncthreads();
    if (tid < 64) {
      float s = 0.f;
#pragma unroll
      for (int w = 0; w < 7; ++w) s += *(const float*)(smem + (w * 64 + tid) * 4);
      const int jj = tid >> 4, row = tid & 15;
      *(float*)(ws + OFF_CSGU + (size_t)kh * 16384 + (size_t)(((blk * 4 + jj) * 2 + which) * 16 + row) * 4) = s;
    }
  } else if (b < 126) {
    const int t = b - 112, nblk = t >> 1;
    if (wave < 7) {
      const int s = (t & 1) * 7 + wave;
      int rows[8];
#pragma unroll
      for (int e = 0; e < 8; ++e) rows[e] = 32 * s + 8 * g + e;
      u32x4 o[4];
      tile_block(wo, D_, nblk * 64, rows, ws + OFF_WO + (size_t)((nblk * 14 + s) * 4) * 1024, 1024, lane, o);
    }
  } else if (b < 182) {
    const int t = b - 126, nblk = t >> 3;
    if (wave < 7) {
      const int s = (t & 7) * 7 + wave;
      const int Fi = 2 * s + (g >> 1);
      int rows[8];
#pragma unroll
      for (int e = 0; e < 8; ++e) rows[e] = 64 * (Fi >> 2) + 16 * (2 * (g & 1) + (e >> 2)) + 4 * (e & 3) + (Fi & 3);
      u32x4 o[4];
      tile_block(wd, D_, nblk * 64, rows, ws + OFF_WD + (size_t)((nblk * 56 + s) * 4) * 1024, 1024, lane, o);
    }
  } else if (b < 214) {
    const int head = (b - 182) >> 1, kh = (b - 182) & 1;
    u32x2 v[11];
#pragma unroll
    for (int it = 0; it < 11; ++it) {
      const int c = it * 512 + tid;
      const int row = c / 24, grp = c - row * 24;
      const int part = grp >> 3, gi = grp & 7;
      v[it] = u32x2{0, 0};
      if (gi < 7 && row < 224) v[it] = *(const u32x2*)(wqkv + (size_t)(kh * 224 + row) * (3 * D_) + part * D_ + head * DH + gi * 4);
    }
#pragma unroll
    for (int it = 0; it < 11; ++it) {
      const int c = it * 512 + tid;
      const int row = c / 24, grp = c - row * 24;
      int pp = (grp >> 2) + ((row >> 3) & 1);
      if (pp >= 6) pp -= 6;
      if (row < 224) *(u32x2*)(smem + row * 192 + pp * 32 + (grp & 3) * 8) = v[it];
    }
    __syncthreads();
    if (wave < 6) {
      const int c = wave;
      const int q4 = (lane & 15) >> 2, p4 = lane & 3;
      int pp = c + (g & 1);
      if (pp >= 6) pp -= 6;
      const char* base = smem + (8 * g + q4) * 192 + pp * 32 + p4 * 8;
      f32x4 cs = {0, 0, 0, 0};
#pragma unroll
      for (int s = 0; s < 7; ++s) {
        u32x4 wf = tr_frag(base + s * 32 * 192, base + s * 32 * 192 + 4 * 192);
        *(u32x4*)(ws + OFF_W1 + (size_t)((head * 14 + kh * 7 + s) * 6 + c) * 1024 + lane * 16) = wf;
        cs = mfma16(wf, ones, cs);
      }
      if ((lane & 15) == 0) *(f32x4*)(ws + OFF_CS1 + (size_t)kh * 8192 + (size_t)(head * 96 + c * 16 + 4 * g) * 4) = cs;
    }
  } else if (b < 246) {
    const int u = (b - 214) * 8 + wave, tf = u >> 1, nb0 = (u & 1) * 4, tok = lane & 15;
    u32x4 a[4][2];
#pragma unroll
    for (int i = 0; i < 4; ++i) {
      const int nb = (nb0 + i) < 7 ? nb0 + i : 6;
#pragma unroll
      for (int h = 0; h < 2; ++h) a[i][h] = *(const u32x4*)(x + (size_t)(tf * 16 + tok) * D_ + nb * 64 + 16 * g + 8 * h);
    }
#pragma unroll
    for (int i = 0; i < 4; ++i) {
      const int nb = nb0 + i;
      if (nb < 7) {
#pragma unroll
        for (int h = 0; h < 2; ++h)
          *(u32x4*)(ws + OFF_X + (size_t)((tf * KS + 2 * nb + (g >> 1)) * 64 + (2 * (g & 1) + h) * 16 + tok) * 16) = a[i][h];
        f32x2 st = stat_partial(a[i][0], a[i][1]);
        if (g == 0) *(f32x2*)(ws + OFF_STX + (size_t)((tf * 16 + tok) * 8 + nb) * 8) = st;
      }
    }
  } else {
    for (unsigned o = tid * 16; o < FLAG_BYTES; o += 512 * 16) *(u32x4*)(ws + o) = u32x4{0, 0, 0, 0};
  }
}

#define MAPMODE 2
static constexpr int LDS_XA = 57344;
static constexpr int LDS_KV = 90112;
static constexpr int LDS_P3 = 139264;
static constexpr int LDS_TAB = 155648;
static constexpr int LDS_EV = 162816;
static constexpr int LDS_TOTAL = 163840;

struct Ctx {
  __amdgpu_buffer_rsrc_t rs;
  char* ws;
};
__device__ __forceinline__ u32x4 ld_sc1(const Ctx& c, unsigned voff, unsigned soff) { return __builtin_amdgcn_raw_buffer_load_b128(c.rs, voff, soff, 16); }
__device__ __forceinline__ u32x4 ld_w(const Ctx& c, unsigned voff, unsigned soff) { return __builtin_amdgcn_raw_buffer_load_b128(c.rs, voff, soff, 0); }
__device__ __forceinline__ void st_sc1(const Ctx& c, unsigned voff, unsigned soff, u32x4 v) { __builtin_amdgcn_raw_buffer_store_b128(v, c.rs, voff, soff, 16); }
__device__ __forceinline__ void st_sc1_b64(const Ctx& c, unsigned voff, unsigned soff, u32x2 v) { __builtin_amdgcn_raw_buffer_store_b64(v, c.rs, voff, soff, 16); }
template <int AUX>
__device__ __forceinline__ void dma(const Ctx& c, unsigned voff, unsigned soff, char* lds_uniform) {
  __builtin_amdgcn_raw_ptr_buffer_load_lds(c.rs, (LAS void*)lds_uniform, 16, voff, soff, 0, AUX);
}
__device__ __forceinline__ void signal_add(const Ctx& c, unsigned off) {
  __hip_atomic_fetch_add((gu32*)(c.ws + off), 1u, __ATOMIC_RELAXED, __HIP_MEMORY_SCOPE_AGENT);
}
__device__ __forceinline__ void poll_ge(const Ctx& c, unsigned off, unsigned target) {
  gu32* f = (gu32*)(c.ws + off);
  gu32* tmo = (gu32*)(c.ws + OFF_TMO);
  unsigned spins = 0;
  while (__hip_atomic_load(f, __ATOMIC_RELAXED, __HIP_MEMORY_SCOPE_AGENT) < target) {
    ++spins;
    if ((spins & 1023u) == 0u) {
      if (spins > (1u << 20) || __hip_atomic_load(tmo, __ATOMIC_RELAXED, __HIP_MEMORY_SCOPE_AGENT) != 0u) {
        __hip_atomic_store(tmo, 1u, __ATOMIC_RELAXED, __HIP_MEMORY_SCOPE_AGENT);
        break;
      }
    }
    __builtin_amdgcn_s_sleep(1);
  }
  __builtin_amdgcn_fence(__ATOMIC_ACQUIRE, "wavefront");
}
__device__ __forceinline__ unsigned cnt_off(int L, int ph, int p, int h) { return OFF_CNT + (unsigned)((((L * 4 + ph) * 16 + p) * 2 + h) * 128); }
__device__ __forceinline__ unsigned kvf_off(int L, int p, int h) { return OFF_KVF + (unsigned)(((L * 16 + p) * 16 + h) * 128); }

template <bool MASKALL>
__device__ __forceinline__ void attn_body(const Ctx& cx, const char* kv, u32x4 qf, int w, int lane, int p, unsigned ooff, int head) {
  const int tok = lane & 15, g = lane >> 4;
  const int t0 = w & ~1;
  const char* kl = kv + t0 * 1024 + lane * 16;
  f32x4 sc[18];
  {
    u32x4 kf[2][3];
#pragma unroll
    for (int q = 0; q < 3; ++q) kf[0][q] = *(const u32x4*)(kl + q * 1024);
#pragma unroll
    for (int gi = 0; gi < 6; ++gi) {
      if (gi + 1 < 6) {
#pragma unroll
        for (int q = 0; q < 3; ++q) kf[(gi + 1) & 1][q] = *(const u32x4*)(kl + ((gi + 1) * 3 + q) * 1024);
      }
#pragma unroll
      for (int q = 0; q < 3; ++q) sc[gi * 3 + q] = mfma16(kf[gi & 1][q], qf, f32x4{0, 0, 0, 0});
      SB();
    }
  }
  const int dq = 16 * (16 + w) + tok - (16 * t0 + 4 * g);
  const int kneg = 16 * (16 - 8 * p);
  const float NEG = -INFINITY;
#pragma unroll
  for (int i = 0; i < 18; ++i) {
    if (MASKALL || i < 2 || i >= 16) {
#pragma unroll
      for (int r = 0; r < 4; ++r) {
        int d = dq - 16 * i - r;
        bool ok = (unsigned)d < 256u;
        if (MASKALL) ok = ok && (16 * (t0 + i) + 4 * g + r >= kneg);
        sc[i][r] = ok ? sc[i][r] : NEG;
      }
    }
  }
  float m = NEG;
#pragma unroll
  for (int i = 0; i < 18; ++i) m = fmaxf(m, fmaxf(fmaxf(sc[i][0], sc[i][1]), fmaxf(sc[i][2], sc[i][3])));
  m = red_max_g(m);
  const float cs = 0.18898223650461362f * 1.4426950408889634f;
  const float mc = m * cs;
  u32x4 pf[9];
#pragma unroll
  for (int i = 0; i < 18; ++i) {
    float e0 = __builtin_amdgcn_exp2f(fmaf(sc[i][0], cs, -mc));
    float e1 = __builtin_amdgcn_exp2f(fmaf(sc[i][1], cs, -mc));
    float e2 = __builtin_amdgcn_exp2f(fmaf(sc[i][2], cs, -mc));
    float e3 = __builtin_amdgcn_exp2f(fmaf(sc[i][3], cs, -mc));
    pf[i >> 1][(i & 1) * 2 + 0] = pk_bf16(e0, e1);
    pf[i >> 1][(i & 1) * 2 + 1] = pk_bf16(e2, e3);
  }
  const int q4 = (lane & 15) >> 2, p4 = lane & 3;
  const char* vl = kv + 24 * 1024 + t0 * 1024;
  const int vb = p4 * 256 + (((4 * g + q4) * 16 + (p4 >> 1) * 128) & 255);
  const int vofs[2] = {vb + (p4 & 1) * 8, vb + ((p4 & 1) ^ 1) * 8};
  f32x4 o[2] = {f32x4{0, 0, 0, 0}, f32x4{0, 0, 0, 0}};
  SB();
  {
    u32x4 vf[3][2];
    auto rdv = [&](int bb) {
#pragma unroll
      for (int f = 0; f < 2; ++f) vf[bb % 3][f] = tr_frag(vl + vofs[f] + (2 * bb) * 1024, vl + vofs[f] + (2 * bb + 1) * 1024);
    };
    rdv(0); rdv(1);
#pragma unroll
    for (int bb = 0; bb < 9; ++bb) {
      if (bb + 2 < 9) rdv(bb + 2);
#pragma unroll
      for (int f = 0; f < 2; ++f) o[f] = mfma16(vf[bb % 3][f], pf[bb], o[f]);
      SB();
    }
  }
  const float lsum = __shfl(o[1][0], 48 + tok);
  const float inv = 1.0f / lsum;
#pragma unroll
  for (int f = 0; f < 2; ++f) {
    if (f == 0 || g < 3) {
      const int k0 = DH * head + 16 * f + 4 * g;
      u32x2 st = {pk_bf16(o[f][0] * inv, o[f][1] * inv), pk_bf16(o[f][2] * inv, o[f][3] * inv)};
      st_sc1_b64(cx, (unsigned)(((k0 >> 5) * 64 + ((k0 & 31) >> 3) * 16 + tok) * 16 + (k0 & 7) * 2), ooff, st);
    }
  }
}

__device__ __forceinline__ void proj_finish(const f32x4 (&fin)[4], const u32x4 (&rs)[2], u32x4 (&o)[2]) {
#pragma unroll
  for (int h = 0; h < 2; ++h)
#pragma unroll
    for (int d = 0; d < 4; ++d) {
      const int c0 = 8 * h + 2 * d;
      unsigned y = pk_bf16(fin[c0 & 3][c0 >> 2], fin[(c0 + 1) & 3][(c0 + 1) >> 2]);
      o[h][d] = pk_bf16(bf_lo(rs[h][d]) + bf_lo(y), bf_hi(rs[h][d]) + bf_hi(y));
    }
}

enum { E_W1, E_XR0, E_XR1, E_KVD0, E_KVD1, E_KVR, E_WO, E_OD0, E_OD1, E_OR0, E_OR1, E_P3P0, E_P3P1, E_P3P2, E_P3P3,
       E_X1D0, E_X1D1, E_X1F0, E_X1R0, E_X1R1, E_HD0, E_HD1, E_HR0, E_HR1, E_T4D, E_P5P0, E_P5P1, E_P5F, E_X2D, E_COUNT };
__device__ __forceinline__ unsigned ev_arrive(char* smem, int e, int lane, unsigned n = 1) {
  asm volatile("s_waitcnt lgkmcnt(0)" ::: "memory");
  unsigned old = 0;
  if (lane == 0) old = __hip_atomic_fetch_add((LAS unsigned*)(smem + LDS_EV + e * 4), n, __ATOMIC_RELAXED, __HIP_MEMORY_SCOPE_WORKGROUP);
  return __builtin_amdgcn_readfirstlane(old);
}
__device__ __forceinline__ unsigned ev_peek(char* smem, int e) {
  unsigned v = __hip_atomic_load((LAS unsigned*)(smem + LDS_EV + e * 4), __ATOMIC_RELAXED, __HIP_MEMORY_SCOPE_WORKGROUP);
  return __builtin_amdgcn_readfirstlane(v);
}
__device__ __forceinline__ void ev_wait(char* smem, int e, unsigned target) {
  while (ev_peek(smem, e) < target) __builtin_amdgcn_s_sleep(1);
  asm volatile("" ::: "memory");
  SB();
}

__device__ __forceinline__ void t5_finalize(const Ctx& cx, char* smem, char* out, int L, int t, int tf, int nblk, const u32x4 (&r5)[2],
                                            int lane, unsigned xlane) {
  const int tok = lane & 15, g = lane >> 4;
  f32x4 fin[4];
#pragma unroll
  for (int jj = 0; jj < 4; ++jj) fin[jj] = *(const f32x4*)(smem + LDS_KV + (t * 4 + jj) * 1024 + lane * 16);
#pragma unroll
  for (int sw = 1; sw < 7; ++sw)
#pragma unroll
    for (int jj = 0; jj < 4; ++jj) fin[jj] += *(const f32x4*)(smem + LDS_KV + ((sw * 2 + t) * 4 + jj) * 1024 + lane * 16);
  u32x4 o[2];
  proj_finish(fin, r5, o);
  if (L == 0) {
    const unsigned xs = (unsigned)(tf * KS + 2 * nblk) * 1024;
    f32x2 st = stat_partial(o[0], o[1]);
    st_sc1(cx, xlane, OFF_X + XT_BYTES + xs, o[0]);
    st_sc1(cx, xlane + 256, OFF_X + XT_BYTES + xs, o[1]);
    if (g == 0) st_sc1_b64(cx, (unsigned)(tok * 64), OFF_STX + ST_BYTES + (unsigned)(tf * 1024 + nblk * 8), __builtin_bit_cast(u32x2, st));
  } else {
    char* op = out + ((size_t)(tf * 16 + tok) * D_ + nblk * 64 + 16 * g) * 2;
    *(u32x4*)op = o[0];
    *(u32x4*)(op + 16) = o[1];
  }
}

__global__ __launch_bounds__(512, 2) void k_main(char* __restrict__ ws_, char* __restrict__ out) {
  extern __shared__ __attribute__((aligned(16))) char smem[];
  Ctx cx;
  cx.ws = ws_;
  cx.rs = __builtin_amdgcn_make_buffer_rsrc(ws_, 0, 0x7fffffff, 0x00020000);
  if (threadIdx.x < 64) *(unsigned*)(smem + LDS_EV + threadIdx.x * 4) = 0u;
  BAR_LDS();

#pragma unroll 1
  for (int L = 0; L < 2; ++L) {
    int tid = threadIdx.x, b = blockIdx.x;
    asm volatile("" : "+v"(tid));
    asm volatile("" : "+s"(b));
    const int lane = tid & 63, wave = __builtin_amdgcn_readfirstlane(tid >> 6);
    const int xcd = b & 7, rr = b >> 3;
    const int p = MAPMODE == 0 ? xcd * 2 + (rr >> 4) : MAPMODE == 1 ? (xcd & 3) * 4 + (rr >> 3) : (xcd & 1) * 8 + (rr >> 2);
    const int j = MAPMODE == 0 ? (rr & 15) : MAPMODE == 1 ? (xcd >> 2) * 8 + (rr & 7) : (xcd >> 1) * 4 + (rr & 3);
    const int tok = lane & 15, g = lane >> 4;
    const bool projwg = (j & 7) < 7;
    const int qq = j >> 3, nblk = projwg ? (j & 7) : 6;
    const unsigned l16 = lane * 16;
    const unsigned xlane = (unsigned)((g >> 1) * 1024 + ((2 * (g & 1)) * 16 + tok) * 16);
    const unsigned stlane = (unsigned)(tok * 64 + g * 16);
    const int tfw = p * 8 + wave;
    const int h = wave >> 2, wl = wave & 3;
    const unsigned M1 = (unsigned)(L + 1);
    STAMP(0);
    if (L == 0) {
      if (wave < 7) {
#pragma unroll
        for (int i = 0; i < 12; ++i) dma<0>(cx, l16, OFF_W1 + (unsigned)(j * 84 + wave * 12 + i) * 1024, smem + (wave * 12 + i) * 1024);
      }
      SB();
    } else {
      if (wave == 0 || wave == 7) {
        if (lane == 0) poll_ge(cx, cnt_off(L - 1, 3, p, h), 14);
        ev_arrive(smem, E_XR0 + h, lane);
      }
      ev_wait(smem, E_XR0 + h, (unsigned)L);
    }
    STAMP(1);
    u32x4 qf;
    {
      u32x4 a[KS];
#pragma unroll
      for (int s = 0; s < KS; ++s) a[s] = ld_sc1(cx, l16, OFF_X + L * XT_BYTES + (unsigned)(tfw * KS + s) * 1024);
      u32x4 straw = ld_sc1(cx, stlane, OFF_STX + L * ST_BYTES + (unsigned)(tfw * 1024));
      u32x4 csr[6][2];
#pragma unroll
      for (int c = 0; c < 6; ++c) {
        csr[c][0] = ld_w(cx, g * 16, OFF_CS1 + (unsigned)(j * 96 + c * 16) * 4);
        csr[c][1] = ld_w(cx, g * 16, OFF_CS1 + 8192 + (unsigned)(j * 96 + c * 16) * 4);
      }
      SB();
      if (L == 0 && wave < 7) {
        WAIT_VM(27);
        ev_arrive(smem, E_W1, lane);
      }
      ev_wait(smem, E_W1, 7 * M1);
      STAMP(2);
      f32x4 acc[6];
#pragma unroll
      for (int c = 0; c < 6; ++c) acc[c] = f32x4{0, 0, 0, 0};
      u32x4 wb[2][6];
#pragma unroll
      for (int c = 0; c < 6; ++c) wb[0][c] = *(const u32x4*)(smem + c * 1024 + l16);
#pragma unroll
      for (int s = 0; s < KS; ++s) {
        if (s + 1 < KS) {
#pragma unroll
          for (int c = 0; c < 6; ++c) wb[(s + 1) & 1][c] = *(const u32x4*)(smem + ((s + 1) * 6 + c) * 1024 + l16);
        }
#pragma unroll
        for (int c = 0; c < 6; ++c) acc[c] = mfma16(wb[s & 1][c], a[s], acc[c]);
        SB();
      }
      STAMP(3);
      const f32x2 st = stat_combine(straw, g);
      unsigned pk[6][2];
#pragma unroll
      for (int c = 0; c < 6; ++c) {
        float v[4];
#pragma unroll
        for (int r = 0; r < 4; ++r)
          v[r] = (acc[c][r] - st[0] * (__uint_as_float(csr[c][0][r]) + __uint_as_float(csr[c][1][r]))) * st[1];
        if (c == 5 && g == 3) v[0] = 1.0f;
        pk[c][0] = pk_bf16(v[0], v[1]);
        pk[c][1] = pk_bf16(v[2], v[3]);
      }
      qf = u32x4{pk[0][0], pk[0][1], pk[1][0], pk[1][1]};
      u32x4 kf = {pk[2][0], pk[2][1], pk[3][0], pk[3][1]};
      u32x4 vf = (g & 1) ? u32x4{pk[5][0], pk[5][1], pk[4][0], pk[4][1]} : u32x4{pk[4][0], pk[4][1], pk[5][0], pk[5][1]};
      const unsigned vslot = (unsigned)(g * 256 + ((tok * 16 + (g >> 1) * 128) & 255));
      st_sc1(cx, l16, OFF_K + L * KV_BYTES + (unsigned)(j * 128 + tfw) * 1024, kf);
      st_sc1(cx, vslot, OFF_V + L * KV_BYTES + (unsigned)(j * 128 + tfw) * 1024, vf);
      if (L > 0 && projwg) ev_wait(smem, E_X2D, 2u * L);
      *(u32x4*)(smem + LDS_KV + (16 + wave) * 1024 + l16) = kf;
      *(u32x4*)(smem + LDS_KV + (24 + 16 + wave) * 1024 + vslot) = vf;
      WAIT_VM(0);
      if (ev_arrive(smem, E_KVD0 + h, lane) == 4 * M1 - 1) {
        if (lane == 0) signal_add(cx, kvf_off(L, p, j));
      }
    }
    STAMP(4);
    const int Fi = j * 7 + (wave < 7 ? wave : 0);
    constexpr int W4E = 12;
    u32x4 w4[KS][2];
    if (wave >= 1 && wave < 7) {
#pragma unroll
      for (int s = 0; s < W4E; ++s) {
        w4[s][0] = ld_w(cx, l16, OFF_WGU + (unsigned)((Fi * KS + s) * 2) * 1024);
        w4[s][1] = ld_w(cx, l16, OFF_WGU + (unsigned)((Fi * KS + s) * 2 + 1) * 1024);
      }
    } else {
#pragma unroll
      for (int s = 0; s < W4E; ++s) { w4[s][0] = u32x4{0, 0, 0, 0}; w4[s][1] = u32x4{0, 0, 0, 0}; }
      if (wave == 0) {
        if (lane == 0) {
          if (p >= 1) poll_ge(cx, kvf_off(L, p - 1, j), 2);
          if (p >= 2) poll_ge(cx, kvf_off(L, p - 2, j), 2);
        }
        SB();
#pragma unroll
        for (int ti = 0; ti < 16; ++ti) {
          const int T = p * 8 - 16 + ti;
          if (T >= 0) {
            dma<16>(cx, l16, OFF_K + L * KV_BYTES + (unsigned)(j * 128 + T) * 1024, smem + LDS_KV + ti * 1024);
            dma<16>(cx, l16, OFF_V + L * KV_BYTES + (unsigned)(j * 128 + T) * 1024, smem + LDS_KV + (24 + ti) * 1024);
          } else {
            *(u32x4*)(smem + LDS_KV + ti * 1024 + l16) = u32x4{0, 0, 0, 0};
            *(u32x4*)(smem + LDS_KV + (24 + ti) * 1024 + l16) = u32x4{0, 0, 0, 0};
          }
        }
        WAIT_VM(0);
        ev_arrive(smem, E_KVR, lane);
      } else if (projwg) {
        ev_wait(smem, E_KVD0, 4 * M1);
        ev_wait(smem, E_KVD1, 4 * M1);
#pragma unroll
        for (int i = 0; i < 56; ++i) dma<0>(cx, l16, OFF_WO + (unsigned)(nblk * 56 + i) * 1024, smem + i * 1024);
      }
    }
    SB();
    ev_wait(smem, E_KVD0, 4 * M1);
    if (h) ev_wait(smem, E_KVD1, 4 * M1);
    ev_wait(smem, E_KVR, M1);
    STAMP(5);
    if (h == 0) __builtin_amdgcn_s_setprio(2);
    {
      const unsigned ooff = OFF_O + L * XT_BYTES + (unsigned)(tfw * KS) * 1024;
      if (p >= 2) attn_body<false>(cx, smem + LDS_KV, qf, wave, lane, p, ooff, j);
      else attn_body<true>(cx, smem + LDS_KV, qf, wave, lane, p, ooff, j);
    }
    __builtin_amdgcn_s_setprio(0);
    STAMP(6);
    WAIT_VM(0);
    if (ev_arrive(smem, E_OD0 + h, lane) == 4 * M1 - 1) {
      if (lane == 0) signal_add(cx, cnt_off(L, 0, p, h));
    }
    if (wave == 7) ev_arrive(smem, E_WO, lane);
    STAMP(7);
    {
      if (projwg) {
        if (wave == 0 || wave == 7) {
          if (lane == 0) poll_ge(cx, cnt_off(L, 0, p, h), 16);
          ev_arrive(smem, E_OR0 + h, lane);
        }
        ev_wait(smem, E_OR0 + h, M1);
      }
      STAMP(8);
      const int t3 = wl & 1, kh = 1 - (wl >> 1);
      const int tf3 = p * 8 + h * 4 + qq * 2 + t3;
      const unsigned xs3 = (unsigned)(tf3 * KS + 2 * nblk) * 1024;
      u32x4 a3[7], rs[2];
      const unsigned abase = (projwg ? OFF_O : OFF_X) + L * XT_BYTES + (unsigned)(tf3 * KS + kh * 7) * 1024;
#pragma unroll
      for (int i = 0; i < 7; ++i) a3[i] = ld_sc1(cx, l16, abase + i * 1024);
      rs[0] = ld_sc1(cx, xlane, OFF_X + L * XT_BYTES + xs3);
      rs[1] = ld_sc1(cx, xlane + 256, OFF_X + L * XT_BYTES + xs3);
      SB();
      if (wave == 0) {
#pragma unroll
        for (int s = 0; s < W4E; ++s) {
          w4[s][0] = ld_w(cx, l16, OFF_WGU + (unsigned)((Fi * KS + s) * 2) * 1024);
          w4[s][1] = ld_w(cx, l16, OFF_WGU + (unsigned)((Fi * KS + s) * 2 + 1) * 1024);
        }
      }
#pragma unroll
      for (int s = W4E; s < KS; ++s) {
        w4[s][0] = ld_w(cx, l16, OFF_WGU + (unsigned)((Fi * KS + s) * 2) * 1024);
        w4[s][1] = ld_w(cx, l16, OFF_WGU + (unsigned)((Fi * KS + s) * 2 + 1) * 1024);
      }
      SB();
      if (projwg) {
        ev_wait(smem, E_WO, M1);
        f32x4 acc[4];
#pragma unroll
        for (int jj = 0; jj < 4; ++jj) acc[jj] = f32x4{0, 0, 0, 0};
        const char* wlp = smem + kh * 28 * 1024 + l16;
#pragma unroll
        for (int i = 0; i < 7; ++i)
#pragma unroll
          for (int jj = 0; jj < 4; ++jj) {
            u32x4 wf = *(const u32x4*)(wlp + (i * 4 + jj) * 1024);
            acc[jj] = mfma16(wf, a3[i], acc[jj]);
          }
        char* pp3 = smem + LDS_P3 + (h * 2 + t3) * 4096 + l16;
        if (kh == 1) {
#pragma unroll
          for (int jj = 0; jj < 4; ++jj) *(f32x4*)(pp3 + jj * 1024) = acc[jj];
          ev_arrive(smem, E_P3P0 + h * 2 + t3, lane);
        } else {
          ev_wait(smem, E_P3P0 + h * 2 + t3, M1);
#pragma unroll
          for (int jj = 0; jj < 4; ++jj) acc[jj] += *(const f32x4*)(pp3 + jj * 1024);
          u32x4 o[2];
          proj_finish(acc, rs, o);
          f32x2 st = stat_partial(o[0], o[1]);
          st_sc1(cx, xlane, OFF_X1 + L * XT_BYTES + xs3, o[0]);
          st_sc1(cx, xlane + 256, OFF_X1 + L * XT_BYTES + xs3, o[1]);
          if (g == 0) st_sc1_b64(cx, (unsigned)(tok * 64), OFF_STX1 + L * ST_BYTES + (unsigned)(tf3 * 1024 + nblk * 8), __builtin_bit_cast(u32x2, st));
          WAIT_VM(0);
          if (ev_arrive(smem, E_X1D0 + h, lane) == 2 * M1 - 1) {
            if (lane == 0) signal_add(cx, cnt_off(L, 1, p, h));
          }
        }
      }
    }
    STAMP(9);
    const int tf5 = p * 8 + qq * 2;
    if (wave < 7) {
      {
        u32x4 c0 = ld_w(cx, g * 16, OFF_CSGU + (unsigned)(Fi * 2 + 0) * 64), c1 = ld_w(cx, g * 16, OFF_CSGU + 16384 + (unsigned)(Fi * 2 + 0) * 64);
        u32x4 c2 = ld_w(cx, g * 16, OFF_CSGU + (unsigned)(Fi * 2 + 1) * 64), c3 = ld_w(cx, g * 16, OFF_CSGU + 16384 + (unsigned)(Fi * 2 + 1) * 64);
        if (h == 0) {
          if (wave == 0) {
            if (lane == 0) poll_ge(cx, cnt_off(L, 1, p, 0), 14);
            ev_wait(smem, E_OD1, 4 * M1);
            ev_arrive(smem, E_X1F0, lane);
          }
          ev_wait(smem, E_X1F0, M1);
          STAMP(10);
#pragma unroll
          for (int s = 0; s < KS; ++s) dma<16>(cx, l16, OFF_X1 + L * XT_BYTES + (unsigned)(tfw * KS + s) * 1024, smem + LDS_XA + (wl * KS + s) * 1024);
          SB();
          u32x4 straw = ld_sc1(cx, stlane, OFF_STX1 + L * ST_BYTES + (unsigned)(tfw * 1024));
          const f32x2 st = stat_combine(straw, g);
          if (g == 0) *(f32x2*)(smem + LDS_TAB + (wave * 16 + tok) * 8) = st;
          WAIT_VM(0);
          ev_arrive(smem, E_X1R0, lane);
        }
        f32x4 sg = __builtin_bit_cast(f32x4, c0) + __builtin_bit_cast(f32x4, c1), su = __builtin_bit_cast(f32x4, c2) + __builtin_bit_cast(f32x4, c3);
        if (tok == 0) {
          *(f32x4*)(smem + LDS_TAB + 1024 + wave * 128 + g * 16) = sg;
          *(f32x4*)(smem + LDS_TAB + 1024 + wave * 128 + 64 + g * 16) = su;
        }
      }
      u32x4 w5[8][4];
      const unsigned w5base = OFF_WD + (unsigned)((nblk * 56 + wave * 8) * 4) * 1024;
      bool h0_signalled = false;
#pragma unroll
      for (int hh = 0; hh < 2; ++hh) {
        if (hh == 1 && ev_peek(smem, E_X1R1) < M1) {
          WAIT_VM(0);
          if (ev_arrive(smem, E_HD0, lane) == 7 * M1 - 1) { if (lane == 0) signal_add(cx, cnt_off(L, 2, p, 0)); }
          h0_signalled = true;
        }
        ev_wait(smem, E_X1R0 + hh, hh == 0 ? 4 * M1 : M1);
        STAMP(11 + hh * 2);
        const char* xt = smem + (hh == 0 ? LDS_XA : 0) + l16;
        f32x4 acc[4][2];
#pragma unroll
        for (int t = 0; t < 4; ++t) { acc[t][0] = f32x4{0, 0, 0, 0}; acc[t][1] = f32x4{0, 0, 0, 0}; }
        u32x4 xb[3][4];
        auto rd = [&](int s) {
#pragma unroll
          for (int t = 0; t < 4; ++t) xb[s % 3][t] = *(const u32x4*)(xt + (t * KS + s) * 1024);
        };
        rd(0); rd(1);
        SB();
#pragma unroll
        for (int s = 0; s < KS; ++s) {
          if (s + 2 < KS) rd(s + 2);
#pragma unroll
          for (int t = 0; t < 4; ++t) {
            acc[t][0] = mfma16(w4[s][0], xb[s % 3][t], acc[t][0]);
            acc[t][1] = mfma16(w4[s][1], xb[s % 3][t], acc[t][1]);
          }
          SB();
          if (hh == 0 && s < 10) { w5[s >> 2][s & 3] = ld_w(cx, l16, w5base + s * 1024); SB(); }
          if (hh == 1 && s < 11) {
#pragma unroll
            for (int q = 0; q < 2; ++q) { const int idx = 10 + s * 2 + q; w5[idx >> 2][idx & 3] = ld_w(cx, l16, w5base + idx * 1024); }
            SB();
          }
          if (hh == 1 && s == 1 && !h0_signalled) {
            WAIT_VM(4);
            if (ev_arrive(smem, E_HD0, lane) == 7 * M1 - 1) { if (lane == 0) signal_add(cx, cnt_off(L, 2, p, 0)); }
            SB();
          }
        }
        STAMP(12 + hh * 2);
        const f32x4 cg = *(const f32x4*)(smem + LDS_TAB + 1024 + wave * 128 + g * 16);
        const f32x4 cu = *(const f32x4*)(smem + LDS_TAB + 1024 + wave * 128 + 64 + g * 16);
        unsigned hv[4][2];
#pragma unroll
        for (int t = 0; t < 4; ++t) {
          const f32x2 s2 = *(const f32x2*)(smem + LDS_TAB + ((hh * 4 + t) * 16 + tok) * 8);
          float gv[4], uv[4];
#pragma unroll
          for (int r = 0; r < 4; ++r) {
            gv[r] = (acc[t][0][r] - s2[0] * cg[r]) * s2[1];
            uv[r] = (acc[t][1][r] - s2[0] * cu[r]) * s2[1];
          }
          unsigned g0 = pk_bf16(gv[0], gv[1]), g1 = pk_bf16(gv[2], gv[3]);
          unsigned u0 = pk_bf16(uv[0], uv[1]), u1 = pk_bf16(uv[2], uv[3]);
          hv[t][0] = pk_bf16(bf_lo(g0) * bf_lo(u0), bf_hi(g0) * bf_hi(u0));
          hv[t][1] = pk_bf16(bf_lo(g1) * bf_lo(u1), bf_hi(g1) * bf_hi(u1));
        }
        const unsigned hlane = (unsigned)((g & 1) * (KSF * 1024) + ((Fi & 1) * 2 + (g >> 1)) * 256 + tok * 16);
#pragma unroll
        for (int u = 0; u < 2; ++u) {
          auto s0 = __builtin_amdgcn_permlane16_swap(hv[2 * u][0], hv[2 * u + 1][0], false, false);
          auto s1 = __builtin_amdgcn_permlane16_swap(hv[2 * u][1], hv[2 * u + 1][1], false, false);
          u32x4 o = {s0[0], s1[0], s0[1], s1[1]};
          st_sc1(cx, hlane, OFF_H + L * H_BYTES + (unsigned)((p * 8 + hh * 4 + 2 * u) * KSF + (Fi >> 1)) * 1024, o);
        }
        SB();
      }
      WAIT_VM(0);
      if (ev_arrive(smem, E_HD1, lane) == 7 * M1 - 1) { if (lane == 0) signal_add(cx, cnt_off(L, 2, p, 1)); }
      ev_arrive(smem, E_T4D, lane);
      STAMP(15);
      if (projwg) {
        u32x4 r5[2];
#pragma unroll
        for (int hh = 0; hh < 2; ++hh) {
          ev_wait(smem, E_HR0 + hh, M1);
          STAMP(16 + hh * 3);
          u32x4 hb[8][2];
          const unsigned hbase = OFF_H + L * H_BYTES + (unsigned)((tf5 + hh * 4) * KSF + wave * 8) * 1024;
#pragma unroll
          for (int i = 0; i < 8; ++i)
#pragma unroll
            for (int t = 0; t < 2; ++t) hb[i][t] = ld_sc1(cx, l16, hbase + (unsigned)(t * KSF + i) * 1024);
          if (hh == 1) {
            const unsigned xs = (unsigned)((tf5 + 4 + (wave & 1)) * KS + 2 * nblk) * 1024;
            r5[0] = ld_sc1(cx, xlane, OFF_X1 + L * XT_BYTES + xs);
            r5[1] = ld_sc1(cx, xlane + 256, OFF_X1 + L * XT_BYTES + xs);
          }
          SB();
          f32x4 acc[4][2];
#pragma unroll
          for (int jj = 0; jj < 4; ++jj) { acc[jj][0] = f32x4{0, 0, 0, 0}; acc[jj][1] = f32x4{0, 0, 0, 0}; }
#pragma unroll
          for (int i = 0; i < 8; ++i)
#pragma unroll
            for (int jj = 0; jj < 4; ++jj)
#pragma unroll
              for (int t = 0; t < 2; ++t) acc[jj][t] = mfma16(w5[i][jj], hb[i][t], acc[jj][t]);
          STAMP(17 + hh * 3);
          if (hh == 0) ev_wait(smem, E_T4D, 7 * M1); else ev_wait(smem, E_P5F, M1);
#pragma unroll
          for (int t = 0; t < 2; ++t)
#pragma unroll
            for (int jj = 0; jj < 4; ++jj) *(f32x4*)(smem + LDS_KV + ((wave * 2 + t) * 4 + jj) * 1024 + l16) = acc[jj][t];
          ev_arrive(smem, E_P5P0 + hh, lane);
          STAMP(18 + hh * 3);
        }
        if (wave < 2) {
          ev_wait(smem, E_P5P1, 7 * M1);
          t5_finalize(cx, smem, out, L, wave, tf5 + 4 + wave, nblk, r5, lane, xlane);
          WAIT_VM(0);
          if (ev_arrive(smem, E_X2D, lane) == 2 * M1 - 1) { if (lane == 0 && L == 0) signal_add(cx, cnt_off(L, 3, p, 1)); }
        }
        STAMP(22);
      }
    } else {
      if (lane == 0) poll_ge(cx, cnt_off(L, 1, p, 1), 14);
      if (projwg) ev_wait(smem, E_X1D0, 2 * M1);
      SB();
      STAMP(10);
#pragma unroll
      for (int i = 0; i < 56; ++i) dma<16>(cx, l16, OFF_X1 + L * XT_BYTES + (unsigned)((p * 8 + 4) * KS + i) * 1024, smem + i * 1024);
      SB();
      {
        u32x4 straw[4];
#pragma unroll
        for (int t = 0; t < 4; ++t) straw[t] = ld_sc1(cx, stlane, OFF_STX1 + L * ST_BYTES + (unsigned)((p * 8 + 4 + t) * 1024));
#pragma unroll
        for (int t = 0; t < 4; ++t) {
          const f32x2 st = stat_combine(straw[t], g);
          if (g == 0) *(f32x2*)(smem + LDS_TAB + ((4 + t) * 16 + tok) * 8) = st;
        }
      }
      WAIT_VM(0);
      ev_arrive(smem, E_X1R1, lane);
      STAMP(11);
      u32x4 r5[2][2];
#pragma unroll
      for (int t = 0; t < 2; ++t) {
        const unsigned xs = (unsigned)((tf5 + t) * KS + 2 * nblk) * 1024;
        r5[t][0] = ld_sc1(cx, xlane, OFF_X1 + L * XT_BYTES + xs);
        r5[t][1] = ld_sc1(cx, xlane + 256, OFF_X1 + L * XT_BYTES + xs);
      }
      if (projwg) {
        if (lane == 0) poll_ge(cx, cnt_off(L, 2, p, 0), 16);
        ev_arrive(smem, E_HR0, lane);
      }
      STAMP(12);
      if (L == 0) {
        ev_wait(smem, E_T4D, 7 * M1);
#pragma unroll
        for (int i = 0; i < 84; ++i) dma<0>(cx, l16, OFF_W1 + (unsigned)(j * 84 + i) * 1024, smem + i * 1024);
        SB();
      }
      STAMP(13);
      if (projwg) {
        if (lane == 0) poll_ge(cx, cnt_off(L, 2, p, 1), 16);
        ev_arrive(smem, E_HR1, lane);
        STAMP(14);
        ev_wait(smem, E_P5P0, 7 * M1);
        t5_finalize(cx, smem, out, L, 0, tf5, nblk, r5[0], lane, xlane);
        t5_finalize(cx, smem, out, L, 1, tf5 + 1, nblk, r5[1], lane, xlane);
        ev_arrive(smem, E_P5F, lane);
        WAIT_VM(0);
        if (lane == 0 && L == 0) signal_add(cx, cnt_off(L, 3, p, 0));
        STAMP(15);
      }
      WAIT_VM(0);
      if (L == 0) ev_arrive(smem, E_W1, lane, 7);
    }
  }
}

extern "C" void kernel_launch(void* const* d_in, const int* in_sizes, int n_in, void* d_out, int out_size, void* d_ws,
                              size_t ws_size, hipStream_t stream) {
  const uint16_t* x = (const uint16_t*)d_in[0];
  const uint16_t* wqkv = (const uint16_t*)d_in[1];
  const uint16_t* wo = (const uint16_t*)d_in[2];
  const uint16_t* wg = (const uint16_t*)d_in[4];
  const uint16_t* wu = (const uint16_t*)d_in[5];
  const uint16_t* wd = (const uint16_t*)d_in[6];
  char* ws = (char*)d_ws;
  constexpr int LP = 224 * 192;
  (void)hipFuncSetAttribute((const void*)k_main, hipFuncAttributeMaxDynamicSharedMemorySize, LDS_TOTAL);
  k_prep<<<247, 512, LP, stream>>>(x, wqkv, wo, wg, wu, wd, ws);
  k_main<<<256, 512, LDS_TOTAL, stream>>>(ws, (char*)d_out);
}
```

```cpp
#include <hip/hip_runtime.h>
#include <hip/hip_bf16.h>
#include <stdint.h>

typedef __attribute__((ext_vector_type(8))) short s16x8;
typedef __attribute__((ext_vector_type(4))) short s16x4;
typedef __attribute__((ext_vector_type(4))) float f32x4;
typedef __attribute__((ext_vector_type(2))) float f32x2;
typedef __attribute__((ext_vector_type(2))) __bf16 bf16x2;
typedef __attribute__((ext_vector_type(4))) unsigned u32x4;
typedef __attribute__((ext_vector_type(2))) unsigned u32x2;
#define LAS __attribute__((address_space(3)))
typedef __attribute__((address_space(1))) unsigned gu32;

static constexpr int S_ = 2048, D_ = 448, F_ = 1792, NH = 16, DH = 28;
static constexpr int KS = 14, KSF = 56;

static constexpr unsigned OFF_CNT = 0;
static constexpr unsigned OFF_KVF = 32768;
static constexpr unsigned OFF_TMO = 98304;
static constexpr unsigned FLAG_BYTES = 98304 + 1024;
static constexpr unsigned OFF_W1 = 131072;
static constexpr unsigned OFF_CS1 = OFF_W1 + 16 * 14 * 6 * 1024;
static constexpr unsigned OFF_WO = OFF_CS1 + 16384;
static constexpr unsigned OFF_WGU = OFF_WO + 7 * 14 * 4 * 1024;
static constexpr unsigned OFF_CSGU = OFF_WGU + 112 * 14 * 2 * 1024;
static constexpr unsigned OFF_WD = OFF_CSGU + 32768;
static constexpr unsigned XT_BYTES = 128 * 14 * 1024;
static constexpr unsigned OFF_X = OFF_WD + 7 * 56 * 4 * 1024;
static constexpr unsigned OFF_X1 = OFF_X + 2 * XT_BYTES;
static constexpr unsigned OFF_O = OFF_X1 + 2 * XT_BYTES;
static constexpr unsigned KV_BYTES = 16 * 128 * 1024;
static constexpr unsigned OFF_K = OFF_O + 2 * XT_BYTES;
static constexpr unsigned OFF_V = OFF_K + 2 * KV_BYTES;
static constexpr unsigned H_BYTES = 128 * 56 * 1024;
static constexpr unsigned OFF_H = OFF_V + 2 * KV_BYTES;
static constexpr unsigned ST_BYTES = 2048 * 64;
static constexpr unsigned OFF_STX = OFF_H + 2 * H_BYTES;
static constexpr unsigned OFF_STX1 = OFF_STX + 2 * ST_BYTES;

__device__ __forceinline__ float bf_lo(unsigned u) { return __uint_as_float(u << 16); }
__device__ __forceinline__ float bf_hi(unsigned u) { return __uint_as_float(u & 0xffff0000u); }
__device__ __forceinline__ unsigned pk_bf16(float a, float b) {
  f32x2 f = {a, b};
  bf16x2 r = __builtin_convertvector(f, bf16x2);
  return __builtin_bit_cast(unsigned, r);
}
__device__ __forceinline__ float rnd_bf16(float a) { return bf_lo(pk_bf16(a, 0.f)); }
__device__ __forceinline__ float red_add_g(float v) {
  auto p = __builtin_amdgcn_permlane16_swap(__float_as_uint(v), __float_as_uint(v), false, false);
  v = __uint_as_float(p[0]) + __uint_as_float(p[1]);
  auto q = __builtin_amdgcn_permlane32_swap(__float_as_uint(v), __float_as_uint(v), false, false);
  return __uint_as_float(q[0]) + __uint_as_float(q[1]);
}
__device__ __forceinline__ float red_max_g(float v) {
  auto p = __builtin_amdgcn_permlane16_swap(__float_as_uint(v), __float_as_uint(v), false, false);
  v = fmaxf(__uint_as_float(p[0]), __uint_as_float(p[1]));
  auto q = __builtin_amdgcn_permlane32_swap(__float_as_uint(v), __float_as_uint(v), false, false);
  return fmaxf(__uint_as_float(q[0]), __uint_as_float(q[1]));
}
__device__ __forceinline__ f32x4 mfma16(u32x4 a, u32x4 b, f32x4 c) {
  return __builtin_amdgcn_mfma_f32_16x16x32_bf16(__builtin_bit_cast(s16x8, a), __builtin_bit_cast(s16x8, b), c, 0, 0, 0);
}
__device__ __forceinline__ u32x4 tr_frag(const char* p0, const char* p1) {
  s16x4 lo = __builtin_amdgcn_ds_read_tr16_b64_v4i16((LAS s16x4*)p0);
  s16x4 hi = __builtin_amdgcn_ds_read_tr16_b64_v4i16((LAS s16x4*)p1);
  u32x2 l2 = __builtin_bit_cast(u32x2, lo), h2 = __builtin_bit_cast(u32x2, hi);
  return u32x4{l2[0], l2[1], h2[0], h2[1]};
}
#define SB() __builtin_amdgcn_sched_barrier(0)
#define STAMP(i)
#define WAIT_VM(n) asm volatile("s_waitcnt vmcnt(" #n ")" ::: "memory")
#define BAR_LDS() do { asm volatile("s_waitcnt lgkmcnt(0)" ::: "memory"); __builtin_amdgcn_s_barrier(); asm volatile("" ::: "memory"); } while (0)

__device__ __forceinline__ f32x2 stat_finish(float sum, f32x4 gram, int tok, int g) {
  float d = (tok & 2) ? ((tok & 1) ? gram[3] : gram[2]) : ((tok & 1) ? gram[1] : gram[0]);
  d = (g == (tok >> 2)) ? d : 0.f;
  const float sq = red_add_g(d);
  const float mean = sum * (1.0f / D_);
  const float var = fmaxf(fmaf(-mean, mean, sq * (1.0f / D_)), 0.f);
  const float mb = rnd_bf16(mean);
  const float den = rnd_bf16(__builtin_amdgcn_sqrtf(rnd_bf16(rnd_bf16(var) + 1e-5f)));
  return f32x2{mb, __builtin_amdgcn_rcpf(den)};
}

__device__ __forceinline__ void tile_block(const uint16_t* __restrict__ src, int ld, int col0, const int (&rows)[8], char* dst,
                                           int jjstride, int lane, u32x4 (&o)[4], bool consec = false) {
  const int i = lane & 15;
  u32x2 v[8];
#pragma unroll
  for (int e = 0; e < 8; ++e) v[e] = *(const u32x2*)(src + (size_t)rows[e] * ld + col0 + 4 * i);
#pragma unroll
  for (int jj = 0; jj < 4; ++jj) {
#pragma unroll
    for (int d = 0; d < 4; ++d)
      o[jj][d] = __builtin_amdgcn_perm(v[2 * d + 1][jj >> 1], v[2 * d][jj >> 1], (jj & 1) ? 0x07060302u : 0x05040100u);
    if (consec) *(u32x4*)(dst + (size_t)(i >> 2) * jjstride + ((lane >> 4) * 16 + 4 * (i & 3) + jj) * 16) = o[jj];
    else *(u32x4*)(dst + (size_t)jj * jjstride + lane * 16) = o[jj];
  }
}

__global__ __launch_bounds__(512, 2) void k_prep(const uint16_t* __restrict__ x, const uint16_t* __restrict__ wqkv,
                                                 const uint16_t* __restrict__ wo, const uint16_t* __restrict__ wg,
                                                 const uint16_t* __restrict__ wu, const uint16_t* __restrict__ wd,
                                                 char* __restrict__ ws) {
  extern __shared__ __attribute__((aligned(16))) char smem[];
  const int tid = threadIdx.x, lane = tid & 63, wave = __builtin_amdgcn_readfirstlane(tid >> 6);
  const int b = blockIdx.x;
  const int g = lane >> 4;
  const u32x4 ones = {0x3f803f80u, 0x3f803f80u, 0x3f803f80u, 0x3f803f80u};
  if (b < 112) {
    const int kh = b & 1, which = (b >> 1) & 1, blk = b >> 2;
    if (wave < 7) {
      const int s = kh * 7 + wave;
      int rows[8];
#pragma unroll
      for (int e = 0; e < 8; ++e) rows[e] = 32 * s + 8 * g + e;
      u32x4 o[4];
      tile_block(which ? wu : wg, F_, blk * 64, rows, ws + OFF_WGU + (size_t)(((blk * 4) * 14 + s) * 2 + which) * 1024,
                 14 * 2 * 1024, lane, o);
#pragma unroll
      for (int jj = 0; jj < 4; ++jj) {
        f32x4 cs = mfma16(o[jj], ones, f32x4{0, 0, 0, 0});
        if ((lane & 15) == 0) *(f32x4*)(smem + ((wave * 4 + jj) * 4 + g) * 16) = cs;
      }
    }
    __syncthreads();
    if (tid < 64) {
      float s = 0.f;
#pragma unroll
      for (int w = 0; w < 7; ++w) s += *(const float*)(smem + (w * 64 + tid) * 4);
      const int jj = tid >> 4, row = tid & 15;
      *(float*)(ws + OFF_CSGU + (size_t)kh * 16384 + (size_t)(((blk * 4 + jj) * 2 + which) * 16 + row) * 4) = s;
    }
  } else if (b < 126) {
    const int t = b - 112, nblk = t >> 1;
    if (wave < 7) {
      const int s = (t & 1) * 7 + wave;
      int rows[8];
#pragma unroll
      for (int e = 0; e < 8; ++e) rows[e] = 32 * s + 8 * g + e;
      u32x4 o[4];
      tile_block(wo, D_, nblk * 64, rows, ws + OFF_WO + (size_t)((nblk * 14 + s) * 4) * 1024, 1024, lane, o, true);
    }
  } else if (b < 182) {
    const int t = b - 126, nblk = t >> 3;
    if (wave < 7) {
      const int s = (t & 7) * 7 + wave;
      const int Fi = 2 * s + (g >> 1);
      int rows[8];
#pragma unroll
      for (int e = 0; e < 8; ++e) rows[e] = 64 * (Fi >> 2) + 16 * (2 * (g & 1) + (e >> 2)) + 4 * (e & 3) + (Fi & 3);
      u32x4 o[4];
      tile_block(wd, D_, nblk * 64, rows, ws + OFF_WD + (size_t)((nblk * 56 + s) * 4) * 1024, 1024, lane, o, true);
    }
  } else if (b < 214) {
    const int head = (b - 182) >> 1, kh = (b - 182) & 1;
    u32x2 v[11];
#pragma unroll
    for (int it = 0; it < 11; ++it) {
      const int c = it * 512 + tid;
      const int row = c / 24, grp = c - row * 24;
      const int part = grp >> 3, gi = grp & 7;
      v[it] = u32x2{grp == 7 ? 0x00003f80u : 0u, 0u};
      if (gi < 7 && row < 224) v[it] = *(const u32x2*)(wqkv + (size_t)(kh * 224 + row) * (3 * D_) + part * D_ + head * DH + gi * 4);
    }
#pragma unroll
    for (int it = 0; it < 11; ++it) {
      const int c = it * 512 + tid;
      const int row = c / 24, grp = c - row * 24;
      int pp = (grp >> 2) + ((row >> 3) & 1);
      if (pp >= 6) pp -= 6;
      if (row < 224) *(u32x2*)(smem + row * 192 + pp * 32 + (grp & 3) * 8) = v[it];
    }
    __syncthreads();
    if (wave < 6) {
      const int c = wave;
      const int q4 = (lane & 15) >> 2, p4 = lane & 3;
      int pp = c + (g & 1);
      if (pp >= 6) pp -= 6;
      const char* base = smem + (8 * g + q4) * 192 + pp * 32 + p4 * 8;
      f32x4 cs = {0, 0, 0, 0};
#pragma unroll
      for (int s = 0; s < 7; ++s) {
        u32x4 wf = tr_frag(base + s * 32 * 192, base + s * 32 * 192 + 4 * 192);
        *(u32x4*)(ws + OFF_W1 + (size_t)((head * 14 + kh * 7 + s) * 6 + c) * 1024 + lane * 16) = wf;
        cs = mfma16(wf, ones, cs);
      }
      if ((lane & 15) == 0) *(f32x4*)(ws + OFF_CS1 + (size_t)kh * 8192 + (size_t)(head * 96 + c * 16 + 4 * g) * 4) = cs;
    }
  } else {
#pragma unroll 1
    for (unsigned o = tid * 16; o < FLAG_BYTES; o += 512 * 16) *(u32x4*)(ws + o) = u32x4{0, 0, 0, 0};
  }
}

#define MAPMODE 2
static constexpr int LDS_REG = 57344;
static constexpr int LDS_W1B = 114688;
static constexpr int LDS_P3 = 143360;
static constexpr int LDS_TAB = 159744;
static constexpr int LDS_EV = 162816;
static constexpr int LDS_TOTAL = 163840;

struct Ctx {
  __amdgpu_buffer_rsrc_t rs;
  char* ws;
};
__device__ __forceinline__ u32x4 ld_sc1(const Ctx& c, unsigned voff, unsigned soff) { return __builtin_amdgcn_raw_buffer_load_b128(c.rs, voff, soff, 16); }
__device__ __forceinline__ u32x4 ld_w(const Ctx& c, unsigned voff, unsigned soff) { return __builtin_amdgcn_raw_buffer_load_b128(c.rs, voff, soff, 0); }
__device__ __forceinline__ void st_sc1(const Ctx& c, unsigned voff, unsigned soff, u32x4 v) { __builtin_amdgcn_raw_buffer_store_b128(v, c.rs, voff, soff, 16); }
__device__ __forceinline__ void st_sc1_b64(const Ctx& c, unsigned voff, unsigned soff, u32x2 v) { __builtin_amdgcn_raw_buffer_store_b64(v, c.rs, voff, soff, 16); }
template <int AUX>
__device__ __forceinline__ void dma(const Ctx& c, unsigned voff, unsigned soff, char* lds_uniform) {
  __builtin_amdgcn_raw_ptr_buffer_load_lds(c.rs, (LAS void*)lds_uniform, 16, voff, soff, 0, AUX);
}
__device__ __forceinline__ void signal_add(const Ctx& c, unsigned off) {
  __hip_atomic_fetch_add((gu32*)(c.ws + off), 1u, __ATOMIC_RELAXED, __HIP_MEMORY_SCOPE_AGENT);
}
__device__ __forceinline__ void poll_ge(const Ctx& c, unsigned off, unsigned target) {
  gu32* f = (gu32*)(c.ws + off);
  gu32* tmo = (gu32*)(c.ws + OFF_TMO);
  unsigned spins = 0;
  while (__hip_atomic_load(f, __ATOMIC_RELAXED, __HIP_MEMORY_SCOPE_AGENT) < target) {
    ++spins;
    if ((spins & 1023u) == 0u) {
      if (spins > (1u << 20) || __hip_atomic_load(tmo, __ATOMIC_RELAXED, __HIP_MEMORY_SCOPE_AGENT) != 0u) {
        __hip_atomic_store(tmo, 1u, __ATOMIC_RELAXED, __HIP_MEMORY_SCOPE_AGENT);
        break;
      }
    }
    __builtin_amdgcn_s_sleep(1);
  }
  __builtin_amdgcn_fence(__ATOMIC_ACQUIRE, "wavefront");
}
__device__ __forceinline__ unsigned cnt_off(int L, int ph, int p, int h) { return OFF_CNT + (unsigned)((((L * 4 + ph) * 16 + p) * 2 + h) * 128); }
__device__ __forceinline__ unsigned kvf_off(int L, int p, int h) { return OFF_KVF + (unsigned)(((L * 16 + p) * 16 + h) * 128); }

__device__ __forceinline__ float max3f(float a, float b, float c) { float r; asm("v_max3_f32 %0, %1, %2, %3" : "=v"(r) : "v"(a), "v"(b), "v"(c)); return r; }
__device__ __forceinline__ void attn_body(const Ctx& cx, const char* kv, u32x4 qf, int w, int lane, int p, unsigned ooff, int head) {
  const int tok = lane & 15, g = lane >> 4;
  const int t0 = w & ~1;
  const char* kl = kv + t0 * 1024 + lane * 16;
  f32x4 sc[18];
  {
    u32x4 kf[2][3];
#pragma unroll
    for (int q = 0; q < 3; ++q) kf[0][q] = *(const u32x4*)(kl + q * 1024);
#pragma unroll
    for (int gi = 0; gi < 6; ++gi) {
      if (gi + 1 < 6) {
#pragma unroll
        for (int q = 0; q < 3; ++q) kf[(gi + 1) & 1][q] = *(const u32x4*)(kl + ((gi + 1) * 3 + q) * 1024);
      }
#pragma unroll
      for (int q = 0; q < 3; ++q) sc[gi * 3 + q] = mfma16(kf[gi & 1][q], qf, f32x4{0, 0, 0, 0});
      SB();
    }
  }
  asm volatile("s_nop 15\n\ts_nop 15\n\ts_nop 15" ::: "memory");
  SB();
  const int dq = 16 * (16 + w) + tok - (16 * t0 + 4 * g);
  const float NEG = -INFINITY;
#pragma unroll
  for (int i = 0; i < 18; ++i) {
    if (i < 2 || i >= 16) {
#pragma unroll
      for (int r = 0; r < 4; ++r) sc[i][r] = ((unsigned)(dq - 16 * i - r) < 256u) ? sc[i][r] : NEG;
    }
  }
  if (p < 2) {
    const int nneg = 16 - 8 * p - t0;
#pragma unroll
    for (int i = 0; i < 16; ++i)
      if (i < nneg) sc[i] = f32x4{NEG, NEG, NEG, NEG};
  }
  float m0 = max3f(sc[0][0], sc[0][1], sc[0][2]), m1 = max3f(sc[0][3], sc[1][0], sc[1][1]), m2 = max3f(sc[1][2], sc[1][3], sc[1][3]);
#pragma unroll
  for (int i = 2; i < 17; i += 3) {
    m0 = max3f(m0, sc[i][0], sc[i][1]); m1 = max3f(m1, sc[i][2], sc[i][3]);
    m2 = max3f(m2, sc[i + 1][0], sc[i + 1][1]); m0 = max3f(m0, sc[i + 1][2], sc[i + 1][3]);
    m1 = max3f(m1, sc[i + 2][0], sc[i + 2][1]); m2 = max3f(m2, sc[i + 2][2], sc[i + 2][3]);
  }
  m0 = max3f(m0, sc[17][0], sc[17][1]); m1 = max3f(m1, sc[17][2], sc[17][3]);
  float m = max3f(m0, m1, m2);
  {
    auto pp = __builtin_amdgcn_permlane16_swap(__float_as_uint(m), __float_as_uint(m), false, false);
    m = max3f(__uint_as_float(pp[0]), __uint_as_float(pp[1]), __uint_as_float(pp[1]));
    auto qq = __builtin_amdgcn_permlane32_swap(__float_as_uint(m), __float_as_uint(m), false, false);
    m = max3f(__uint_as_float(qq[0]), __uint_as_float(qq[1]), __uint_as_float(qq[1]));
  }
  const float cs = 0.18898223650461362f * 1.4426950408889634f;
  const float mc = m * cs;
  const f32x2 cs2 = {cs, cs}, mc2 = {-mc, -mc};
  u32x4 pf[9];
#pragma unroll
  for (int i = 0; i < 18; ++i) {
    f32x2 x0 = __builtin_elementwise_fma(f32x2{sc[i][0], sc[i][1]}, cs2, mc2);
    f32x2 x1 = __builtin_elementwise_fma(f32x2{sc[i][2], sc[i][3]}, cs2, mc2);
    pf[i >> 1][(i & 1) * 2 + 0] = pk_bf16(__builtin_amdgcn_exp2f(x0[0]), __builtin_amdgcn_exp2f(x0[1]));
    pf[i >> 1][(i & 1) * 2 + 1] = pk_bf16(__builtin_amdgcn_exp2f(x1[0]), __builtin_amdgcn_exp2f(x1[1]));
  }
  const int q4 = (lane & 15) >> 2, p4 = lane & 3;
  const char* vl = kv + 24 * 1024 + t0 * 1024;
  const int vb = p4 * 256 + (((4 * g + q4) * 16 + (p4 >> 1) * 128) & 255);
  const int vofs[2] = {vb + (p4 & 1) * 8, vb + ((p4 & 1) ^ 1) * 8};
  f32x4 o[2] = {f32x4{0, 0, 0, 0}, f32x4{0, 0, 0, 0}};
  SB();
  {
    u32x4 vf[3][2];
    auto rdv = [&](int bb) {
#pragma unroll
      for (int f = 0; f < 2; ++f) vf[bb % 3][f] = tr_frag(vl + vofs[f] + (2 * bb) * 1024, vl + vofs[f] + (2 * bb + 1) * 1024);
    };
    rdv(0); rdv(1);
#pragma unroll
    for (int bb = 0; bb < 9; ++bb) {
      if (bb + 2 < 9) rdv(bb + 2);
#pragma unroll
      for (int f = 0; f < 2; ++f) o[f] = mfma16(vf[bb % 3][f], pf[bb], o[f]);
      SB();
    }
  }
  const float lsum = __shfl(o[1][0], 48 + tok);
  const float inv = __builtin_amdgcn_rcpf(lsum);
#pragma unroll
  for (int f = 0; f < 2; ++f) {
    if (f == 0 || g < 3) {
      const int k0 = DH * head + 16 * f + 4 * g;
      u32x2 st = {pk_bf16(o[f][0] * inv, o[f][1] * inv), pk_bf16(o[f][2] * inv, o[f][3] * inv)};
      st_sc1_b64(cx, (unsigned)(((k0 >> 5) * 64 + ((k0 & 31) >> 3) * 16 + tok) * 16 + (k0 & 7) * 2), ooff, st);
    }
  }
}

enum { E_X1B, E_ST0, E_ST1, E_HD0, E_HD1, E_HR0, E_HR1, E_T4A, E_T4D, E_P5P0, E_P5P1, E_P5F, E_X2D, E_COUNT };
__device__ __forceinline__ unsigned ev_arrive(char* smem, int e, int lane) {
  asm volatile("s_waitcnt lgkmcnt(0)" ::: "memory");
  unsigned old = 0;
  if (lane == 0) old = __hip_atomic_fetch_add((LAS unsigned*)(smem + LDS_EV + e * 4), 1u, __ATOMIC_RELAXED, __HIP_MEMORY_SCOPE_WORKGROUP);
  return __builtin_amdgcn_readfirstlane(old);
}
__device__ __forceinline__ void ev_post(char* smem, int e, int lane) {
  asm volatile("s_waitcnt lgkmcnt(0)" ::: "memory");
  if (lane == 0) __hip_atomic_fetch_add((LAS unsigned*)(smem + LDS_EV + e * 4), 1u, __ATOMIC_RELAXED, __HIP_MEMORY_SCOPE_WORKGROUP);
}
__device__ __forceinline__ unsigned ev_peek(char* smem, int e) {
  unsigned v = __hip_atomic_load((LAS unsigned*)(smem + LDS_EV + e * 4), __ATOMIC_RELAXED, __HIP_MEMORY_SCOPE_WORKGROUP);
  return __builtin_amdgcn_readfirstlane(v);
}
__device__ __forceinline__ void ev_wait(char* smem, int e, unsigned target) {
  while (ev_peek(smem, e) < target) __builtin_amdgcn_s_sleep(1);
  asm volatile("" ::: "memory");
  SB();
}
#define BAR() do { __builtin_amdgcn_s_barrier(); asm volatile("" ::: "memory"); SB(); } while (0)
__device__ __forceinline__ u32x2 add_res(f32x4 y, u32x2 r) {
  const unsigned y0 = pk_bf16(y[0], y[1]), y1 = pk_bf16(y[2], y[3]);
  return u32x2{pk_bf16(bf_lo(r[0]) + bf_lo(y0), bf_hi(r[0]) + bf_hi(y0)), pk_bf16(bf_lo(r[1]) + bf_lo(y1), bf_hi(r[1]) + bf_hi(y1))};
}

__global__ __launch_bounds__(512, 2) void k_main(char* __restrict__ ws_, const char* __restrict__ xin, char* __restrict__ out) {
  extern __shared__ __attribute__((aligned(16))) char smem[];
  Ctx cx;
  cx.ws = ws_;
  cx.rs = __builtin_amdgcn_make_buffer_rsrc(ws_, 0, 0x7fffffff, 0x00020000);
  const __amdgpu_buffer_rsrc_t rx = __builtin_amdgcn_make_buffer_rsrc((void*)xin, 0, S_ * D_ * 2, 0x00020000);
  const __amdgpu_buffer_rsrc_t ro = __builtin_amdgcn_make_buffer_rsrc(out, 0, S_ * D_ * 2, 0x00020000);
  if (threadIdx.x < 64) *(unsigned*)(smem + LDS_EV + threadIdx.x * 4) = 0u;

#pragma unroll 1
  for (int L = 0; L < 2; ++L) {
    int tid = threadIdx.x, b = blockIdx.x;
    asm volatile("" : "+v"(tid));
    asm volatile("" : "+s"(b));
    const int lane = tid & 63, wave = __builtin_amdgcn_readfirstlane(tid >> 6);
    const int xcd = b & 7, rr = b >> 3;
    const int p = MAPMODE == 0 ? xcd * 2 + (rr >> 4) : MAPMODE == 1 ? (xcd & 3) * 4 + (rr >> 3) : (xcd & 1) * 8 + (rr >> 2);
    const int j = MAPMODE == 0 ? (rr & 15) : MAPMODE == 1 ? (xcd >> 2) * 8 + (rr & 7) : (xcd >> 1) * 4 + (rr & 3);
    const int tok = lane & 15, g = lane >> 4;
    const bool projwg = (j & 7) < 7;
    const int qq = j >> 3, nblk = projwg ? (j & 7) : 6;
    const unsigned l16 = lane * 16;
    const unsigned r8t = (unsigned)(((g >> 1) * 16 + tok) * 16 + (g & 1) * 8);
    const unsigned r8r = (unsigned)(tok * (D_ * 2) + g * 8);
    const int tfw = p * 8 + wave;
    const unsigned M1 = (unsigned)(L + 1);
    char* const RW = smem + (L ? 0 : LDS_REG);
    char* const RA = smem + (L ? LDS_REG : 0);
    const __amdgpu_buffer_rsrc_t rsx = L ? cx.rs : rx;
    const unsigned xbase = L ? OFF_X + XT_BYTES : 0u;
    STAMP(0);
    if (L == 0) {
      if (wave < 7) {
#pragma unroll
        for (int i = 0; i < 12; ++i) dma<0>(cx, l16, OFF_W1 + (unsigned)(j * 84 + wave * 12 + i) * 1024, smem + LDS_REG + (wave * 12 + i) * 1024);
      }
      SB();
    } else {
      if (tid == 448) poll_ge(cx, cnt_off(L - 1, 3, p, 0), 14);
      BAR();
    }
    STAMP(1);
    u32x4 qf;
    {
      u32x4 a[KS];
      const unsigned va = L ? l16 : (unsigned)(tok * (D_ * 2) + g * 16), sst = L ? 1024u : 64u;
#pragma unroll
      for (int s = 0; s < KS; ++s) a[s] = __builtin_amdgcn_raw_buffer_load_b128(rsx, va, xbase + (unsigned)tfw * 14336u + s * sst, 16);
      u32x4 csr[6][2];
#pragma unroll
      for (int c = 0; c < 6; ++c) {
        csr[c][0] = ld_w(cx, g * 16, OFF_CS1 + (unsigned)(j * 96 + c * 16) * 4);
        csr[c][1] = ld_w(cx, g * 16, OFF_CS1 + 8192 + (unsigned)(j * 96 + c * 16) * 4);
      }
      SB();
      if (L == 0) {
        WAIT_VM(26);
        BAR_LDS();
        SB();
      }
      STAMP(2);
      f32x4 acc[6], gram = {0, 0, 0, 0};
#pragma unroll
      for (int c = 0; c < 6; ++c) acc[c] = f32x4{0, 0, 0, 0};
      u32x4 wb[2][6];
#pragma unroll
      for (int c = 0; c < 6; ++c) wb[0][c] = *(const u32x4*)(RW + c * 1024 + l16);
#pragma unroll
      for (int s = 0; s < KS; ++s) {
        if (s + 1 < KS) {
#pragma unroll
          for (int c = 0; c < 6; ++c) {
            const int ti = (s + 1) * 6 + c;
            wb[(s + 1) & 1][c] = *(const u32x4*)((ti < 56 ? RW + ti * 1024 : smem + LDS_W1B + (ti - 56) * 1024) + l16);
          }
        }
        gram = mfma16(a[s], a[s], gram);
#pragma unroll
        for (int c = 0; c < 6; ++c) acc[c] = mfma16(wb[s & 1][c], a[s], acc[c]);
        SB();
      }
      STAMP(3);
      const f32x2 st = stat_finish(red_add_g(g == 3 ? acc[1][0] : 0.f), gram, tok, g);
      const float mi = st[0] * st[1];
      unsigned pk[6][2];
#pragma unroll
      for (int c = 0; c < 6; ++c) {
        const f32x4 csum = __builtin_bit_cast(f32x4, csr[c][0]) + __builtin_bit_cast(f32x4, csr[c][1]);
        float v[4];
#pragma unroll
        for (int r = 0; r < 4; ++r) v[r] = fmaf(acc[c][r], st[1], -mi * csum[r]);
        if (c == 1 && g == 3) v[0] = 0.0f;
        if (c == 5 && g == 3) v[0] = 1.0f;
        pk[c][0] = pk_bf16(v[0], v[1]);
        pk[c][1] = pk_bf16(v[2], v[3]);
      }
      qf = u32x4{pk[0][0], pk[0][1], pk[1][0], pk[1][1]};
      u32x4 kf = {pk[2][0], pk[2][1], pk[3][0], pk[3][1]};
      u32x4 vf = (g & 1) ? u32x4{pk[5][0], pk[5][1], pk[4][0], pk[4][1]} : u32x4{pk[4][0], pk[4][1], pk[5][0], pk[5][1]};
      const unsigned vslot = (unsigned)(g * 256 + ((tok * 16 + (g >> 1) * 128) & 255));
      st_sc1(cx, l16, OFF_K + L * KV_BYTES + (unsigned)(j * 128 + tfw) * 1024, kf);
      st_sc1(cx, vslot, OFF_V + L * KV_BYTES + (unsigned)(j * 128 + tfw) * 1024, vf);
      *(u32x4*)(RA + (16 + wave) * 1024 + l16) = kf;
      *(u32x4*)(RA + (24 + 16 + wave) * 1024 + vslot) = vf;
      if (p < 2) {
        u32x4 z = {0, 0, 0, 0};
        asm volatile("" : "+v"(z));
#pragma unroll
        for (int i = 0; i < 2; ++i) {
          const int ti = wave * 2 + i;
          if (p * 8 - 16 + ti < 0) {
            *(u32x4*)(RA + ti * 1024 + l16) = z;
            *(u32x4*)(RA + (24 + ti) * 1024 + l16) = z;
          }
        }
      }
      WAIT_VM(0);
      BAR_LDS();
    }
    STAMP(4);
    if (tid == 448) {
      signal_add(cx, kvf_off(L, p, j));
      if (p >= 1) poll_ge(cx, kvf_off(L, p - 1, j), 1);
      if (p >= 2) poll_ge(cx, kvf_off(L, p - 2, j), 1);
    }
    BAR();
#pragma unroll
    for (int i = 0; i < 2; ++i) {
      const int ti = wave * 2 + i, T = p * 8 - 16 + ti;
      if (T >= 0) {
        dma<16>(cx, l16, OFF_K + L * KV_BYTES + (unsigned)(j * 128 + T) * 1024, RA + ti * 1024);
        dma<16>(cx, l16, OFF_V + L * KV_BYTES + (unsigned)(j * 128 + T) * 1024, RA + (24 + ti) * 1024);
      }
    }
    WAIT_VM(0);
    BAR();
    STAMP(5);
    const int Fi = j * 7 + (wave < 7 ? wave : 0);
    constexpr int W4E = 12;
    u32x4 w4[KS][2];
    if (wave < 7) {
#pragma unroll
      for (int s = 0; s < W4E; ++s) {
        w4[s][0] = ld_w(cx, l16, OFF_WGU + (unsigned)((Fi * KS + s) * 2) * 1024);
        w4[s][1] = ld_w(cx, l16, OFF_WGU + (unsigned)((Fi * KS + s) * 2 + 1) * 1024);
      }
    } else {
#pragma unroll
      for (int s = 0; s < W4E; ++s) { w4[s][0] = u32x4{0, 0, 0, 0}; w4[s][1] = u32x4{0, 0, 0, 0}; }
      if (projwg) {
#pragma unroll
        for (int i = 0; i < 56; ++i) dma<0>(cx, l16, OFF_WO + (unsigned)(nblk * 56 + i) * 1024, RW + i * 1024);
      }
    }
    SB();
    attn_body(cx, RA, qf, wave, lane, p, OFF_O + L * XT_BYTES + (unsigned)(tfw * KS) * 1024, j);
    STAMP(6);
    WAIT_VM(0);
    BAR();
    STAMP(7);
    if (tid == 448) {
      signal_add(cx, cnt_off(L, 0, p, 0));
      if (projwg) poll_ge(cx, cnt_off(L, 0, p, 0), 16);
    }
    if (projwg) {
      const int t3 = wave & 3, kh = wave >> 2;
      const int tf3 = p * 8 + qq * 4 + t3;
      const char* wlp = RW + kh * 28 * 1024 + l16;
      u32x4 wf[3][4];
#pragma unroll
      for (int i = 0; i < 2; ++i)
#pragma unroll
        for (int jj = 0; jj < 4; ++jj) wf[i][jj] = *(const u32x4*)(wlp + (i * 4 + jj) * 1024);
      u32x2 rs[2];
#pragma unroll
      for (int q = 0; q < 2; ++q) {
        const int jj = kh * 2 + q;
        const unsigned so = L ? (unsigned)(tf3 * KS + 2 * nblk + kh) * 1024 + q * 512 : (unsigned)tf3 * 14336u + nblk * 128 + jj * 32;
        rs[q] = __builtin_amdgcn_raw_buffer_load_b64(rsx, L ? r8t : r8r, xbase + so, 16);
      }
      BAR();
      STAMP(8);
      u32x4 a3[7];
#pragma unroll
      for (int i = 0; i < 7; ++i) a3[i] = ld_sc1(cx, l16, OFF_O + L * XT_BYTES + (unsigned)(tf3 * KS + kh * 7 + i) * 1024);
      SB();
      f32x4 acc[4];
#pragma unroll
      for (int jj = 0; jj < 4; ++jj) acc[jj] = f32x4{0, 0, 0, 0};
#pragma unroll
      for (int i = 0; i < 7; ++i) {
        if (i + 2 < 7) {
#pragma unroll
          for (int jj = 0; jj < 4; ++jj) wf[(i + 2) % 3][jj] = *(const u32x4*)(wlp + ((i + 2) * 4 + jj) * 1024);
        }
#pragma unroll
        for (int jj = 0; jj < 4; ++jj) acc[jj] = mfma16(wf[i % 3][jj], a3[i], acc[jj]);
        SB();
      }
      char* pp3 = smem + LDS_P3 + l16;
      *(f32x4*)(pp3 + (wave * 2 + 0) * 1024) = kh ? acc[0] : acc[2];
      *(f32x4*)(pp3 + (wave * 2 + 1) * 1024) = kh ? acc[1] : acc[3];
      BAR_LDS();
#pragma unroll
      for (int q = 0; q < 2; ++q) {
        f32x4 y = (kh ? acc[2 + q] : acc[q]) + *(const f32x4*)(pp3 + ((wave ^ 4) * 2 + q) * 1024);
        st_sc1_b64(cx, r8t, OFF_X1 + L * XT_BYTES + (unsigned)(tf3 * KS + 2 * nblk + kh) * 1024 + q * 512, add_res(y, rs[q]));
      }
      WAIT_VM(0);
    }
    BAR();
    STAMP(9);
    if (tid == 448) {
      if (projwg) signal_add(cx, cnt_off(L, 1, p, 0));
      poll_ge(cx, cnt_off(L, 1, p, 0), 14);
    }
    if (wave < 7) {
#pragma unroll
      for (int s = W4E; s < KS; ++s) {
        w4[s][0] = ld_w(cx, l16, OFF_WGU + (unsigned)((Fi * KS + s) * 2) * 1024);
        w4[s][1] = ld_w(cx, l16, OFF_WGU + (unsigned)((Fi * KS + s) * 2 + 1) * 1024);
      }
      u32x4 c0 = ld_w(cx, g * 16, OFF_CSGU + (unsigned)(Fi * 2 + 0) * 64), c1 = ld_w(cx, g * 16, OFF_CSGU + 16384 + (unsigned)(Fi * 2 + 0) * 64);
      u32x4 c2 = ld_w(cx, g * 16, OFF_CSGU + (unsigned)(Fi * 2 + 1) * 64), c3 = ld_w(cx, g * 16, OFF_CSGU + 16384 + (unsigned)(Fi * 2 + 1) * 64);
      f32x4 sg = __builtin_bit_cast(f32x4, c0) + __builtin_bit_cast(f32x4, c1), su = __builtin_bit_cast(f32x4, c2) + __builtin_bit_cast(f32x4, c3);
      if (tok == 0) {
        *(f32x4*)(smem + LDS_TAB + 1024 + wave * 128 + g * 16) = sg;
        *(f32x4*)(smem + LDS_TAB + 1024 + wave * 128 + 64 + g * 16) = su;
      }
    } else {
#pragma unroll
      for (int s = W4E; s < KS; ++s) { w4[s][0] = u32x4{0, 0, 0, 0}; w4[s][1] = u32x4{0, 0, 0, 0}; }
    }
    BAR();
    STAMP(10);
#pragma unroll
    for (int i = 0; i < 7; ++i) dma<16>(cx, l16, OFF_X1 + L * XT_BYTES + (unsigned)(p * 8 * KS + wave * 7 + i) * 1024, RA + (wave * 7 + i) * 1024);
    SB();
    asm volatile("" ::: "memory");
#pragma unroll
    for (int i = 0; i < 7; ++i) dma<16>(cx, l16, OFF_X1 + L * XT_BYTES + (unsigned)((p * 8 + 4) * KS + wave * 7 + i) * 1024, RW + (wave * 7 + i) * 1024);
    WAIT_VM(7);
    BAR();
    STAMP(11);
    const int tf5 = p * 8 + qq * 2;
    const int ft = wave >> 2, fj = wave & 3;
    const unsigned fso = (unsigned)(2 * nblk + (fj >> 1)) * 1024 + (fj & 1) * 512;
    u32x4 w5[8][4];
    if (wave < 7) {
      const unsigned w5base = OFF_WD + (unsigned)((nblk * 56 + wave * 8) * 4) * 1024;
#pragma unroll
      for (int hh = 0; hh < 2; ++hh) {
        const char* xt = (hh == 0 ? RA : RW) + l16;
        f32x4 acc[4][2];
#pragma unroll
        for (int t = 0; t < 4; ++t) { acc[t][0] = f32x4{0, 0, 0, 0}; acc[t][1] = f32x4{0, 0, 0, 0}; }
        u32x4 xb[3][4];
        auto rd = [&](int s) {
#pragma unroll
          for (int t = 0; t < 4; ++t) xb[s % 3][t] = *(const u32x4*)(xt + (t * KS + s) * 1024);
        };
        rd(0); rd(1);
        SB();
#pragma unroll
        for (int s = 0; s < KS; ++s) {
          if (s + 2 < KS) rd(s + 2);
#pragma unroll
          for (int t = 0; t < 4; ++t) {
            acc[t][0] = mfma16(w4[s][0], xb[s % 3][t], acc[t][0]);
            acc[t][1] = mfma16(w4[s][1], xb[s % 3][t], acc[t][1]);
          }
          SB();
          if (hh == 0 && s == 3) { WAIT_VM(0); ev_post(smem, E_X1B, lane); SB(); }
          if (hh == 0 && s >= 6) { w5[(s - 6) >> 2][(s - 6) & 3] = ld_w(cx, l16, w5base + (s - 6) * 1024); SB(); }
          if (hh == 1 && s < 12) {
#pragma unroll
            for (int q = 0; q < 2; ++q) { const int idx = 8 + s * 2 + q; w5[idx >> 2][idx & 3] = ld_w(cx, l16, w5base + idx * 1024); }
            SB();
          }
          if (hh == 1 && s == 2) {
            WAIT_VM(6);
            if (ev_arrive(smem, E_HD0, lane) == 7 * M1 - 1) { if (lane == 0) signal_add(cx, cnt_off(L, 2, p, 0)); }
            SB();
          }
        }
        STAMP(12 + hh * 2);
        ev_post(smem, E_T4A + hh, lane);
        ev_wait(smem, E_ST0 + hh, M1);
        const f32x4 cg = *(const f32x4*)(smem + LDS_TAB + 1024 + wave * 128 + g * 16);
        const f32x4 cu = *(const f32x4*)(smem + LDS_TAB + 1024 + wave * 128 + 64 + g * 16);
        unsigned hv[4][2];
#pragma unroll
        for (int t = 0; t < 4; ++t) {
          const f32x2 s2 = *(const f32x2*)(smem + LDS_TAB + ((hh * 4 + t) * 16 + tok) * 8);
          float gv[4], uv[4];
          float nmi = -s2[0], inv = s2[1];
#pragma unroll
          for (int r = 0; r < 4; ++r) {
            float tg = nmi * cg[r], tu = nmi * cu[r];
            asm volatile("" : "+v"(tg), "+v"(tu));
            gv[r] = fmaf(acc[t][0][r], inv, tg);
            uv[r] = fmaf(acc[t][1][r], inv, tu);
            asm volatile("" : "+v"(gv[r]), "+v"(uv[r]));
          }
          unsigned g0 = pk_bf16(gv[0], gv[1]), g1 = pk_bf16(gv[2], gv[3]);
          unsigned u0 = pk_bf16(uv[0], uv[1]), u1 = pk_bf16(uv[2], uv[3]);
          hv[t][0] = pk_bf16(bf_lo(g0) * bf_lo(u0), bf_hi(g0) * bf_hi(u0));
          hv[t][1] = pk_bf16(bf_lo(g1) * bf_lo(u1), bf_hi(g1) * bf_hi(u1));
        }
        const unsigned hlane = (unsigned)((g & 1) * (KSF * 1024) + ((Fi & 1) * 2 + (g >> 1)) * 256 + tok * 16);
#pragma unroll
        for (int u = 0; u < 2; ++u) {
          auto s0 = __builtin_amdgcn_permlane16_swap(hv[2 * u][0], hv[2 * u + 1][0], false, false);
          auto s1 = __builtin_amdgcn_permlane16_swap(hv[2 * u][1], hv[2 * u + 1][1], false, false);
          u32x4 o = {s0[0], s1[0], s0[1], s1[1]};
          st_sc1(cx, hlane, OFF_H + L * H_BYTES + (unsigned)((p * 8 + hh * 4 + 2 * u) * KSF + (Fi >> 1)) * 1024, o);
        }
        SB();
        if (hh == 0) { ev_wait(smem, E_X1B, 8 * M1); STAMP(13); }
      }
      WAIT_VM(0);
      if (ev_arrive(smem, E_HD1, lane) == 7 * M1 - 1) { if (lane == 0) signal_add(cx, cnt_off(L, 2, p, 1)); }
      STAMP(15);
    } else {
#pragma unroll
      for (int i = 0; i < 8; ++i)
#pragma unroll
        for (int jj = 0; jj < 4; ++jj) w5[i][jj] = u32x4{0, 0, 0, 0};
      const u32x4 ones = {0x3f803f80u, 0x3f803f80u, 0x3f803f80u, 0x3f803f80u};
#pragma unroll
      for (int hh = 0; hh < 2; ++hh) {
        if (hh == 1) { WAIT_VM(0); ev_post(smem, E_X1B, lane); ev_wait(smem, E_X1B, 8 * M1); }
        const char* xt = (hh == 0 ? RA : RW) + l16;
        f32x4 sm[4], gr[4];
#pragma unroll
        for (int t = 0; t < 4; ++t) { sm[t] = f32x4{0, 0, 0, 0}; gr[t] = f32x4{0, 0, 0, 0}; }
        u32x4 xb[3][4];
        auto rd = [&](int s) {
#pragma unroll
          for (int t = 0; t < 4; ++t) xb[s % 3][t] = *(const u32x4*)(xt + (t * KS + s) * 1024);
        };
        rd(0); rd(1);
        SB();
#pragma unroll
        for (int s = 0; s < KS; ++s) {
          if (s + 2 < KS) rd(s + 2);
#pragma unroll
          for (int t = 0; t < 4; ++t) {
            sm[t] = mfma16(ones, xb[s % 3][t], sm[t]);
            gr[t] = mfma16(xb[s % 3][t], xb[s % 3][t], gr[t]);
          }
          SB();
        }
#pragma unroll
        for (int t = 0; t < 4; ++t) {
          const f32x2 st = stat_finish(sm[t][0], gr[t], tok, g);
          if (g == 0) *(f32x2*)(smem + LDS_TAB + ((hh * 4 + t) * 16 + tok) * 8) = f32x2{st[0] * st[1], st[1]};
        }
        ev_post(smem, E_ST0 + hh, lane);
        STAMP(12 + hh);
      }
      if (L == 0) {
        ev_wait(smem, E_T4A, 7 * M1);
#pragma unroll 1
        for (int i = 0; i < 84; i += 4) {
#pragma unroll
          for (int q = 0; q < 4; ++q)
            dma<0>(cx, l16, OFF_W1 + (unsigned)(j * 84 + i + q) * 1024, i < 56 ? RA + (i + q) * 1024 : smem + LDS_W1B + (i - 56 + q) * 1024);
        }
        SB();
      }
      STAMP(14);
      if (projwg) {
        if (lane == 0) poll_ge(cx, cnt_off(L, 2, p, 0), 16);
        ev_post(smem, E_HR0, lane);
        STAMP(15);
        if (lane == 0) poll_ge(cx, cnt_off(L, 2, p, 1), 16);
        ev_post(smem, E_HR1, lane);
        STAMP(16);
      }
    }
    if (projwg) {
      u32x2 r5[2];
#pragma unroll
      for (int hh = 0; hh < 2; ++hh)
        r5[hh] = __builtin_amdgcn_raw_buffer_load_b64(cx.rs, r8t, OFF_X1 + L * XT_BYTES + (unsigned)((tf5 + hh * 4 + ft) * KS) * 1024 + fso, 16);
#pragma unroll
      for (int hh = 0; hh < 2; ++hh) {
        if (wave < 7) {
          ev_wait(smem, E_HR0 + hh, M1);
          STAMP(16 + hh * 3);
          u32x4 hb[8][2];
          const unsigned hbase = OFF_H + L * H_BYTES + (unsigned)((tf5 + hh * 4) * KSF + wave * 8) * 1024;
#pragma unroll
          for (int i = 0; i < 8; ++i)
#pragma unroll
            for (int t = 0; t < 2; ++t) hb[i][t] = ld_sc1(cx, l16, hbase + (unsigned)(t * KSF + i) * 1024);
          SB();
          f32x4 acc[4][2];
#pragma unroll
          for (int jj = 0; jj < 4; ++jj) { acc[jj][0] = f32x4{0, 0, 0, 0}; acc[jj][1] = f32x4{0, 0, 0, 0}; }
#pragma unroll
          for (int i = 0; i < 8; ++i)
#pragma unroll
            for (int jj = 0; jj < 4; ++jj)
#pragma unroll
              for (int t = 0; t < 2; ++t) acc[jj][t] = mfma16(w5[i][jj], hb[i][t], acc[jj][t]);
          STAMP(17 + hh * 3);
          if (hh == 0) { ev_wait(smem, E_T4D, 7 * M1); ev_wait(smem, E_ST1, M1); } else ev_wait(smem, E_P5F, 8 * M1);
#pragma unroll
          for (int t = 0; t < 2; ++t)
#pragma unroll
            for (int jj = 0; jj < 4; ++jj) *(f32x4*)(RW + (wave * 8 + t * 4 + jj) * 1024 + l16) = acc[jj][t];
          ev_post(smem, E_P5P0 + hh, lane);
        }
        ev_wait(smem, E_P5P0 + hh, 7 * M1);
        STAMP(18 + hh * 3);
        f32x4 y = *(const f32x4*)(RW + wave * 1024 + l16);
#pragma unroll
        for (int sw = 1; sw < 7; ++sw) y += *(const f32x4*)(RW + (sw * 8 + wave) * 1024 + l16);
        if (hh == 0) ev_post(smem, E_P5F, lane);
        const u32x2 o = add_res(y, r5[hh]);
        const int tf = tf5 + hh * 4 + ft;
        if (L == 0) st_sc1_b64(cx, r8t, OFF_X + XT_BYTES + (unsigned)(tf * KS) * 1024 + fso, o);
        else __builtin_amdgcn_raw_buffer_store_b64(o, ro, r8r, (unsigned)tf * 14336u + nblk * 128 + fj * 32, 0);
      }
      if (L == 0) {
        WAIT_VM(0);
        if (ev_arrive(smem, E_X2D, lane) == 7) { if (lane == 0) signal_add(cx, cnt_off(L, 3, p, 0)); }
      }
      STAMP(22);
    }
    WAIT_VM(0);
  }
}

extern "C" void kernel_launch(void* const* d_in, const int* in_sizes, int n_in, void* d_out, int out_size, void* d_ws,
                              size_t ws_size, hipStream_t stream) {
  const uint16_t* x = (const uint16_t*)d_in[0];
  const uint16_t* wqkv = (const uint16_t*)d_in[1];
  const uint16_t* wo = (const uint16_t*)d_in[2];
  const uint16_t* wg = (const uint16_t*)d_in[4];
  const uint16_t* wu = (const uint16_t*)d_in[5];
  const uint16_t* wd = (const uint16_t*)d_in[6];
  char* ws = (char*)d_ws;
  constexpr int LP = 224 * 192;
  (void)hipFuncSetAttribute((const void*)k_main, hipFuncAttributeMaxDynamicSharedMemorySize, LDS_TOTAL);
  k_prep<<<215, 512, LP, stream>>>(x, wqkv, wo, wg, wu, wd, ws);
  k_main<<<256, 512, LDS_TOTAL, stream>>>(ws, (const char*)x, (char*)d_out);
}
```

```cpp
#include <hip/hip_runtime.h>
#include <hip/hip_bf16.h>
#include <stdint.h>

typedef __attribute__((ext_vector_type(8))) short s16x8;
typedef __attribute__((ext_vector_type(4))) short s16x4;
typedef __attribute__((ext_vector_type(4))) float f32x4;
typedef __attribute__((ext_vector_type(2))) float f32x2;
typedef __attribute__((ext_vector_type(2))) __bf16 bf16x2;
typedef __attribute__((ext_vector_type(4))) unsigned u32x4;
typedef __attribute__((ext_vector_type(2))) unsigned u32x2;
#define LAS __attribute__((address_space(3)))
typedef __attribute__((address_space(1))) unsigned gu32;

static constexpr int S_ = 2048, D_ = 448, F_ = 1792, NH = 16, DH = 28;
static constexpr int KS = 14, KSF = 56;

static constexpr unsigned OFF_CNT = 0;
static constexpr unsigned OFF_KVF = 32768;
static constexpr unsigned OFF_TMO = 98304;
static constexpr unsigned FLAG_BYTES = 98304 + 1024;
static constexpr unsigned OFF_W1 = 131072;
static constexpr unsigned OFF_CS1 = OFF_W1 + 16 * 14 * 6 * 1024;
static constexpr unsigned OFF_WO = OFF_CS1 + 16384;
static constexpr unsigned OFF_WGU = OFF_WO + 7 * 14 * 4 * 1024;
static constexpr unsigned OFF_CSGU = OFF_WGU + 112 * 14 * 2 * 1024;
static constexpr unsigned OFF_WD = OFF_CSGU + 32768;
static constexpr unsigned XT_BYTES = 128 * 14 * 1024;
static constexpr unsigned OFF_X = OFF_WD + 7 * 56 * 4 * 1024;
static constexpr unsigned OFF_X1 = OFF_X + 2 * XT_BYTES;
static constexpr unsigned OFF_O = OFF_X1 + 2 * XT_BYTES;
static constexpr unsigned KV_BYTES = 16 * 128 * 1024;
static constexpr unsigned OFF_K = OFF_O + 2 * XT_BYTES;
static constexpr unsigned OFF_V = OFF_K + 2 * KV_BYTES;
static constexpr unsigned H_BYTES = 128 * 56 * 1024;
static constexpr unsigned OFF_H = OFF_V + 2 * KV_BYTES;
static constexpr unsigned ST_BYTES = 2048 * 64;
static constexpr unsigned OFF_STX = OFF_H + 2 * H_BYTES;
static constexpr unsigned OFF_STX1 = OFF_STX + 2 * ST_BYTES;

__device__ __forceinline__ float bf_lo(unsigned u) { return __uint_as_float(u << 16); }
__device__ __forceinline__ float bf_hi(unsigned u) { return __uint_as_float(u & 0xffff0000u); }
__device__ __forceinline__ unsigned pk_bf16(float a, float b) {
  f32x2 f = {a, b};
  bf16x2 r = __builtin_convertvector(f, bf16x2);
  return __builtin_bit_cast(unsigned, r);
}
__device__ __forceinline__ float rnd_bf16(float a) { return bf_lo(pk_bf16(a, 0.f)); }
__device__ __forceinline__ float red_add_g(float v) {
  auto p = __builtin_amdgcn_permlane16_swap(__float_as_uint(v), __float_as_uint(v), false, false);
  v = __uint_as_float(p[0]) + __uint_as_float(p[1]);
  auto q = __builtin_amdgcn_permlane32_swap(__float_as_uint(v), __float_as_uint(v), false, false);
  return __uint_as_float(q[0]) + __uint_as_float(q[1]);
}
__device__ __forceinline__ float red_max_g(float v) {
  auto p = __builtin_amdgcn_permlane16_swap(__float_as_uint(v), __float_as_uint(v), false, false);
  v = fmaxf(__uint_as_float(p[0]), __uint_as_float(p[1]));
  auto q = __builtin_amdgcn_permlane32_swap(__float_as_uint(v), __float_as_uint(v), false, false);
  return fmaxf(__uint_as_float(q[0]), __uint_as_float(q[1]));
}
__device__ __forceinline__ f32x4 mfma16(u32x4 a, u32x4 b, f32x4 c) {
  return __builtin_amdgcn_mfma_f32_16x16x32_bf16(__builtin_bit_cast(s16x8, a), __builtin_bit_cast(s16x8, b), c, 0, 0, 0);
}
__device__ __forceinline__ u32x4 tr_frag(const char* p0, const char* p1) {
  s16x4 lo = __builtin_amdgcn_ds_read_tr16_b64_v4i16((LAS s16x4*)p0);
  s16x4 hi = __builtin_amdgcn_ds_read_tr16_b64_v4i16((LAS s16x4*)p1);
  u32x2 l2 = __builtin_bit_cast(u32x2, lo), h2 = __builtin_bit_cast(u32x2, hi);
  return u32x4{l2[0], l2[1], h2[0], h2[1]};
}
#define SB() __builtin_amdgcn_sched_barrier(0)
#define STAMP(i)
#define WAIT_VM(n) asm volatile("s_waitcnt vmcnt(" #n ")" ::: "memory")
#define BAR_LDS() do { asm volatile("s_waitcnt lgkmcnt(0)" ::: "memory"); __builtin_amdgcn_s_barrier(); asm volatile("" ::: "memory"); } while (0)

__device__ __forceinline__ f32x2 stat_finish(float sum, f32x4 gram, int tok, int g) {
  float d = (tok & 2) ? ((tok & 1) ? gram[3] : gram[2]) : ((tok & 1) ? gram[1] : gram[0]);
  d = (g == (tok >> 2)) ? d : 0.f;
  const float sq = red_add_g(d);
  const float mean = sum * (1.0f / D_);
  const float var = fmaxf(fmaf(-mean, mean, sq * (1.0f / D_)), 0.f);
  const float mb = rnd_bf16(mean);
  const float den = rnd_bf16(__builtin_amdgcn_sqrtf(rnd_bf16(rnd_bf16(var) + 1e-5f)));
  return f32x2{mb, __builtin_amdgcn_rcpf(den)};
}

__device__ __forceinline__ void tile_block(const uint16_t* __restrict__ src, int ld, int col0, const int (&rows)[8], char* dst,
                                           int jjstride, int lane, u32x4 (&o)[4], bool consec = false) {
  const int i = lane & 15;
  u32x2 v[8];
#pragma unroll
  for (int e = 0; e < 8; ++e) v[e] = *(const u32x2*)(src + (size_t)rows[e] * ld + col0 + 4 * i);
#pragma unroll
  for (int jj = 0; jj < 4; ++jj) {
#pragma unroll
    for (int d = 0; d < 4; ++d)
      o[jj][d] = __builtin_amdgcn_perm(v[2 * d + 1][jj >> 1], v[2 * d][jj >> 1], (jj & 1) ? 0x07060302u : 0x05040100u);
    if (consec) *(u32x4*)(dst + (size_t)(i >> 2) * jjstride + ((lane >> 4) * 16 + 4 * (i & 3) + jj) * 16) = o[jj];
    else *(u32x4*)(dst + (size_t)jj * jjstride + lane * 16) = o[jj];
  }
}

__global__ __launch_bounds__(512, 2) void k_prep(const uint16_t* __restrict__ x, const uint16_t* __restrict__ wqkv,
                                                 const uint16_t* __restrict__ wo, const uint16_t* __restrict__ wg,
                                                 const uint16_t* __restrict__ wu, const uint16_t* __restrict__ wd,
                                                 char* __restrict__ ws) {
  extern __shared__ __attribute__((aligned(16))) char smem[];
  const int tid = threadIdx.x, lane = tid & 63, wave = __builtin_amdgcn_readfirstlane(tid >> 6);
  const int b = blockIdx.x;
  const int g = lane >> 4;
  const u32x4 ones = {0x3f803f80u, 0x3f803f80u, 0x3f803f80u, 0x3f803f80u};
  if (b < 112) {
    const int kh = b & 1, which = (b >> 1) & 1, blk = b >> 2;
    if (wave < 7) {
      const int s = kh * 7 + wave;
      int rows[8];
#pragma unroll
      for (int e = 0; e < 8; ++e) rows[e] = 32 * s + 8 * g + e;
      u32x4 o[4];
      tile_block(which ? wu : wg, F_, blk * 64, rows, ws + OFF_WGU + (size_t)(((blk * 4) * 14 + s) * 2 + which) * 1024,
                 14 * 2 * 1024, lane, o);
#pragma unroll
      for (int jj = 0; jj < 4; ++jj) {
        f32x4 cs = mfma16(o[jj], ones, f32x4{0, 0, 0, 0});
        if ((lane & 15) == 0) *(f32x4*)(smem + ((wave * 4 + jj) * 4 + g) * 16) = cs;
      }
    }
    __syncthreads();
    if (tid < 64) {
      float s = 0.f;
#pragma unroll
      for (int w = 0; w < 7; ++w) s += *(const float*)(smem + (w * 64 + tid) * 4);
      const int jj = tid >> 4, row = tid & 15;
      *(float*)(ws + OFF_CSGU + (size_t)kh * 16384 + (size_t)(((blk * 4 + jj) * 2 + which) * 16 + row) * 4) = s;
    }
  } else if (b < 126) {
    const int t = b - 112, nblk = t >> 1;
    if (wave < 7) {
      const int s = (t & 1) * 7 + wave;
      int rows[8];
#pragma unroll
      for (int e = 0; e < 8; ++e) rows[e] = 32 * s + 8 * g + e;
      u32x4 o[4];
      tile_block(wo, D_, nblk * 64, rows, ws + OFF_WO + (size_t)((nblk * 14 + s) * 4) * 1024, 1024, lane, o, true);
    }
  } else if (b < 182) {
    const int t = b - 126, nblk = t >> 3;
    if (wave < 7) {
      const int s = (t & 7) * 7 + wave;
      const int Fi = 2 * s + (g >> 1);
      int rows[8];
#pragma unroll
      for (int e = 0; e < 8; ++e) rows[e] = 64 * (Fi >> 2) + 16 * (2 * (g & 1) + (e >> 2)) + 4 * (e & 3) + (Fi & 3);
      u32x4 o[4];
      tile_block(wd, D_, nblk * 64, rows, ws + OFF_WD + (size_t)((nblk * 56 + s) * 4) * 1024, 1024, lane, o, true);
    }
  } else if (b < 214) {
    const int head = (b - 182) >> 1, kh = (b - 182) & 1;
    u32x2 v[11];
#pragma unroll
    for (int it = 0; it < 11; ++it) {
      const int c = it * 512 + tid;
      const int row = c / 24, grp = c - row * 24;
      const int part = grp >> 3, gi = grp & 7;
      v[it] = u32x2{grp == 7 ? 0x00003f80u : 0u, 0u};
      if (gi < 7 && row < 224) v[it] = *(const u32x2*)(wqkv + (size_t)(kh * 224 + row) * (3 * D_) + part * D_ + head * DH + gi * 4);
    }
#pragma unroll
    for (int it = 0; it < 11; ++it) {
      const int c = it * 512 + tid;
      const int row = c / 24, grp = c - row * 24;
      int pp = (grp >> 2) + ((row >> 3) & 1);
      if (pp >= 6) pp -= 6;
      if (row < 224) *(u32x2*)(smem + row * 192 + pp * 32 + (grp & 3) * 8) = v[it];
    }
    __syncthreads();
    if (wave < 6) {
      const int c = wave;
      const int q4 = (lane & 15) >> 2, p4 = lane & 3;
      int pp = c + (g & 1);
      if (pp >= 6) pp -= 6;
      const char* base = smem + (8 * g + q4) * 192 + pp * 32 + p4 * 8;
      f32x4 cs = {0, 0, 0, 0};
#pragma unroll
      for (int s = 0; s < 7; ++s) {
        u32x4 wf = tr_frag(base + s * 32 * 192, base + s * 32 * 192 + 4 * 192);
        *(u32x4*)(ws + OFF_W1 + (size_t)((head * 14 + kh * 7 + s) * 6 + c) * 1024 + lane * 16) = wf;
        cs = mfma16(wf, ones, cs);
      }
      if ((lane & 15) == 0) *(f32x4*)(ws + OFF_CS1 + (size_t)kh * 8192 + (size_t)(head * 96 + c * 16 + 4 * g) * 4) = cs;
    }
  } else {
#pragma unroll 1
    for (unsigned o = tid * 16; o < FLAG_BYTES; o += 512 * 16) *(u32x4*)(ws + o) = u32x4{0, 0, 0, 0};
  }
}

#define MAPMODE 2
static constexpr int LDS_REG = 57344;
static constexpr int LDS_W1B = 114688;
static constexpr int LDS_P3 = 143360;
static constexpr int LDS_TAB = 159744;
static constexpr int LDS_EV = 162816;
static constexpr int LDS_TOTAL = 163840;

struct Ctx {
  __amdgpu_buffer_rsrc_t rs;
  char* ws;
};
__device__ __forceinline__ u32x4 ld_sc1(const Ctx& c, unsigned voff, unsigned soff) { return __builtin_amdgcn_raw_buffer_load_b128(c.rs, voff, soff, 16); }
__device__ __forceinline__ u32x4 ld_w(const Ctx& c, unsigned voff, unsigned soff) { return __builtin_amdgcn_raw_buffer_load_b128(c.rs, voff, soff, 0); }
__device__ __forceinline__ void st_sc1(const Ctx& c, unsigned voff, unsigned soff, u32x4 v) { __builtin_amdgcn_raw_buffer_store_b128(v, c.rs, voff, soff, 16); }
__device__ __forceinline__ void st_sc1_b64(const Ctx& c, unsigned voff, unsigned soff, u32x2 v) { __builtin_amdgcn_raw_buffer_store_b64(v, c.rs, voff, soff, 16); }
template <int AUX>
__device__ __forceinline__ void dma(const Ctx& c, unsigned voff, unsigned soff, char* lds_uniform) {
  __builtin_amdgcn_raw_ptr_buffer_load_lds(c.rs, (LAS void*)lds_uniform, 16, voff, soff, 0, AUX);
}
__device__ __forceinline__ void signal_add(const Ctx& c, unsigned off) {
  __hip_atomic_fetch_add((gu32*)(c.ws + off), 1u, __ATOMIC_RELAXED, __HIP_MEMORY_SCOPE_AGENT);
}
__device__ __forceinline__ void poll_ge(const Ctx& c, unsigned off, unsigned target) {
  gu32* f = (gu32*)(c.ws + off);
  gu32* tmo = (gu32*)(c.ws + OFF_TMO);
  unsigned spins = 0;
  while (__hip_atomic_load(f, __ATOMIC_RELAXED, __HIP_MEMORY_SCOPE_AGENT) < target) {
    ++spins;
    if ((spins & 1023u) == 0u) {
      if (spins > (1u << 20) || __hip_atomic_load(tmo, __ATOMIC_RELAXED, __HIP_MEMORY_SCOPE_AGENT) != 0u) {
        __hip_atomic_store(tmo, 1u, __ATOMIC_RELAXED, __HIP_MEMORY_SCOPE_AGENT);
        break;
      }
    }
    __builtin_amdgcn_s_sleep(1);
  }
  __builtin_amdgcn_fence(__ATOMIC_ACQUIRE, "wavefront");
}
__device__ __forceinline__ unsigned cnt_off(int L, int ph, int p, int h) { return OFF_CNT + (unsigned)((((L * 4 + ph) * 16 + p) * 2 + h) * 128); }
__device__ __forceinline__ unsigned kvf_off(int L, int p, int h) { return OFF_KVF + (unsigned)(((L * 16 + p) * 16 + h) * 128); }

__device__ __forceinline__ float max3f(float a, float b, float c) { float r; asm("v_max3_f32 %0, %1, %2, %3" : "=v"(r) : "v"(a), "v"(b), "v"(c)); return r; }
__device__ __forceinline__ void attn_body(const Ctx& cx, const char* kv, u32x4 qf, int w, int lane, int p, unsigned ooff, int head) {
  const int tok = lane & 15, g = lane >> 4;
  const int t0 = w & ~1;
  const char* kl = kv + t0 * 1024 + lane * 16;
  f32x4 sc[18];
  {
    u32x4 kf[2][3];
#pragma unroll
    for (int q = 0; q < 3; ++q) kf[0][q] = *(const u32x4*)(kl + q * 1024);
#pragma unroll
    for (int gi = 0; gi < 6; ++gi) {
      if (gi + 1 < 6) {
#pragma unroll
        for (int q = 0; q < 3; ++q) kf[(gi + 1) & 1][q] = *(const u32x4*)(kl + ((gi + 1) * 3 + q) * 1024);
      }
#pragma unroll
      for (int q = 0; q < 3; ++q) sc[gi * 3 + q] = mfma16(kf[gi & 1][q], qf, f32x4{0, 0, 0, 0});
      SB();
    }
  }
  asm volatile("s_nop 15\n\ts_nop 15\n\ts_nop 15" ::: "memory");
  SB();
  const int dq = 16 * (16 + w) + tok - (16 * t0 + 4 * g);
  const float NEG = -INFINITY;
#pragma unroll
  for (int i = 0; i < 18; ++i) {
    if (i < 2 || i >= 16) {
#pragma unroll
      for (int r = 0; r < 4; ++r) sc[i][r] = ((unsigned)(dq - 16 * i - r) < 256u) ? sc[i][r] : NEG;
    }
  }
  if (p < 2) {
    const int nneg = 16 - 8 * p - t0;
#pragma unroll
    for (int i = 0; i < 16; ++i)
      if (i < nneg) sc[i] = f32x4{NEG, NEG, NEG, NEG};
  }
  float m0 = max3f(sc[0][0], sc[0][1], sc[0][2]), m1 = max3f(sc[0][3], sc[1][0], sc[1][1]), m2 = max3f(sc[1][2], sc[1][3], sc[1][3]);
#pragma unroll
  for (int i = 2; i < 17; i += 3) {
    m0 = max3f(m0, sc[i][0], sc[i][1]); m1 = max3f(m1, sc[i][2], sc[i][3]);
    m2 = max3f(m2, sc[i + 1][0], sc[i + 1][1]); m0 = max3f(m0, sc[i + 1][2], sc[i + 1][3]);
    m1 = max3f(m1, sc[i + 2][0], sc[i + 2][1]); m2 = max3f(m2, sc[i + 2][2], sc[i + 2][3]);
  }
  m0 = max3f(m0, sc[17][0], sc[17][1]); m1 = max3f(m1, sc[17][2], sc[17][3]);
  float m = max3f(m0, m1, m2);
  {
    auto pp = __builtin_amdgcn_permlane16_swap(__float_as_uint(m), __float_as_uint(m), false, false);
    m = max3f(__uint_as_float(pp[0]), __uint_as_float(pp[1]), __uint_as_float(pp[1]));
    auto qq = __builtin_amdgcn_permlane32_swap(__float_as_uint(m), __float_as_uint(m), false, false);
    m = max3f(__uint_as_float(qq[0]), __uint_as_float(qq[1]), __uint_as_float(qq[1]));
  }
  const float cs = 0.18898223650461362f * 1.4426950408889634f;
  const float mc = m * cs;
  const f32x2 cs2 = {cs, cs}, mc2 = {-mc, -mc};
  u32x4 pf[9];
#pragma unroll
  for (int i = 0; i < 18; ++i) {
    f32x2 x0 = __builtin_elementwise_fma(f32x2{sc[i][0], sc[i][1]}, cs2, mc2);
    f32x2 x1 = __builtin_elementwise_fma(f32x2{sc[i][2], sc[i][3]}, cs2, mc2);
    pf[i >> 1][(i & 1) * 2 + 0] = pk_bf16(__builtin_amdgcn_exp2f(x0[0]), __builtin_amdgcn_exp2f(x0[1]));
    pf[i >> 1][(i & 1) * 2 + 1] = pk_bf16(__builtin_amdgcn_exp2f(x1[0]), __builtin_amdgcn_exp2f(x1[1]));
  }
  const int q4 = (lane & 15) >> 2, p4 = lane & 3;
  const char* vl = kv + 24 * 1024 + t0 * 1024;
  const int vb = p4 * 256 + (((4 * g + q4) * 16 + (p4 >> 1) * 128) & 255);
  const int vofs[2] = {vb + (p4 & 1) * 8, vb + ((p4 & 1) ^ 1) * 8};
  f32x4 o[2] = {f32x4{0, 0, 0, 0}, f32x4{0, 0, 0, 0}};
  SB();
  {
    u32x4 vf[3][2];
    auto rdv = [&](int bb) {
#pragma unroll
      for (int f = 0; f < 2; ++f) vf[bb % 3][f] = tr_frag(vl + vofs[f] + (2 * bb) * 1024, vl + vofs[f] + (2 * bb + 1) * 1024);
    };
    rdv(0); rdv(1);
#pragma unroll
    for (int bb = 0; bb < 9; ++bb) {
      if (bb + 2 < 9) rdv(bb + 2);
#pragma unroll
      for (int f = 0; f < 2; ++f) o[f] = mfma16(vf[bb % 3][f], pf[bb], o[f]);
      SB();
    }
  }
  const float lsum = __shfl(o[1][0], 48 + tok);
  const float inv = __builtin_amdgcn_rcpf(lsum);
#pragma unroll
  for (int f = 0; f < 2; ++f) {
    if (f == 0 || g < 3) {
      const int k0 = DH * head + 16 * f + 4 * g;
      u32x2 st = {pk_bf16(o[f][0] * inv, o[f][1] * inv), pk_bf16(o[f][2] * inv, o[f][3] * inv)};
      st_sc1_b64(cx, (unsigned)(((k0 >> 5) * 64 + ((k0 & 31) >> 3) * 16 + tok) * 16 + (k0 & 7) * 2), ooff, st);
    }
  }
}

enum { E_X1B, E_ST0, E_ST1, E_HD0, E_HD1, E_HR0, E_HR1, E_T4A, E_T4D, E_P5P0, E_P5P1, E_P5F, E_X2D, E_COUNT };
__device__ __forceinline__ unsigned ev_arrive(char* smem, int e, int lane) {
  asm volatile("s_waitcnt lgkmcnt(0)" ::: "memory");
  unsigned old = 0;
  if (lane == 0) old = __hip_atomic_fetch_add((LAS unsigned*)(smem + LDS_EV + e * 4), 1u, __ATOMIC_RELAXED, __HIP_MEMORY_SCOPE_WORKGROUP);
  return __builtin_amdgcn_readfirstlane(old);
}
__device__ __forceinline__ void ev_post(char* smem, int e, int lane) {
  asm volatile("s_waitcnt lgkmcnt(0)" ::: "memory");
  if (lane == 0) __hip_atomic_fetch_add((LAS unsigned*)(smem + LDS_EV + e * 4), 1u, __ATOMIC_RELAXED, __HIP_MEMORY_SCOPE_WORKGROUP);
}
__device__ __forceinline__ unsigned ev_peek(char* smem, int e) {
  unsigned v = __hip_atomic_load((LAS unsigned*)(smem + LDS_EV + e * 4), __ATOMIC_RELAXED, __HIP_MEMORY_SCOPE_WORKGROUP);
  return __builtin_amdgcn_readfirstlane(v);
}
__device__ __forceinline__ void ev_wait(char* smem, int e, unsigned target) {
  while (ev_peek(smem, e) < target) __builtin_amdgcn_s_sleep(1);
  asm volatile("" ::: "memory");
  SB();
}
#define BAR() do { __builtin_amdgcn_s_barrier(); asm volatile("" ::: "memory"); SB(); } while (0)
__device__ __forceinline__ u32x2 add_res(f32x4 y, u32x2 r) {
  const unsigned y0 = pk_bf16(y[0], y[1]), y1 = pk_bf16(y[2], y[3]);
  return u32x2{pk_bf16(bf_lo(r[0]) + bf_lo(y0), bf_hi(r[0]) + bf_hi(y0)), pk_bf16(bf_lo(r[1]) + bf_lo(y1), bf_hi(r[1]) + bf_hi(y1))};
}

__global__ __launch_bounds__(512, 2) void k_main(char* __restrict__ ws_, const char* __restrict__ xin, char* __restrict__ out) {
  extern __shared__ __attribute__((aligned(16))) char smem[];
  Ctx cx;
  cx.ws = ws_;
  cx.rs = __builtin_amdgcn_make_buffer_rsrc(ws_, 0, 0x7fffffff, 0x00020000);
  const __amdgpu_buffer_rsrc_t rx = __builtin_amdgcn_make_buffer_rsrc((void*)xin, 0, S_ * D_ * 2, 0x00020000);
  const __amdgpu_buffer_rsrc_t ro = __builtin_amdgcn_make_buffer_rsrc(out, 0, S_ * D_ * 2, 0x00020000);
  if (threadIdx.x < 64) *(unsigned*)(smem + LDS_EV + threadIdx.x * 4) = 0u;

#pragma unroll 1
  for (int L = 0; L < 2; ++L) {
    int tid = threadIdx.x, b = blockIdx.x;
    asm volatile("" : "+v"(tid));
    asm volatile("" : "+s"(b));
    const int lane = tid & 63, wave = __builtin_amdgcn_readfirstlane(tid >> 6);
    const int xcd = b & 7, rr = b >> 3;
    const int p = MAPMODE == 0 ? xcd * 2 + (rr >> 4) : MAPMODE == 1 ? (xcd & 3) * 4 + (rr >> 3) : (xcd & 1) * 8 + (rr >> 2);
    const int j = MAPMODE == 0 ? (rr & 15) : MAPMODE == 1 ? (xcd >> 2) * 8 + (rr & 7) : (xcd >> 1) * 4 + (rr & 3);
    const int tok = lane & 15, g = lane >> 4;
    const bool projwg = (j & 7) < 7;
    const int qq = j >> 3, nblk = projwg ? (j & 7) : 6;
    const unsigned l16 = lane * 16;
    const unsigned r8t = (unsigned)(((g >> 1) * 16 + tok) * 16 + (g & 1) * 8);
    const unsigned r8r = (unsigned)(tok * (D_ * 2) + g * 8);
    const int tfw = p * 8 + wave;
    const unsigned M1 = (unsigned)(L + 1);
    char* const RW = smem + (L ? 0 : LDS_REG);
    char* const RA = smem + (L ? LDS_REG : 0);
    const __amdgpu_buffer_rsrc_t rsx = L ? cx.rs : rx;
    const unsigned xbase = L ? OFF_X + XT_BYTES : 0u;
    STAMP(0);
    if (L == 0) {
      if (wave < 7) {
#pragma unroll
        for (int i = 0; i < 12; ++i) dma<0>(cx, l16, OFF_W1 + (unsigned)(j * 84 + wave * 12 + i) * 1024, smem + LDS_REG + (wave * 12 + i) * 1024);
      }
      SB();
    } else {
      if (tid == 448) poll_ge(cx, cnt_off(L - 1, 3, p, 0), 14);
      BAR();
    }
    STAMP(1);
    u32x4 qf;
    {
      u32x4 a[KS];
      const unsigned va = L ? l16 : (unsigned)(tok * (D_ * 2) + g * 16), sst = L ? 1024u : 64u;
#pragma unroll
      for (int s = 0; s < KS; ++s) a[s] = __builtin_amdgcn_raw_buffer_load_b128(rsx, va, xbase + (unsigned)tfw * 14336u + s * sst, 16);
      SB();
      if (L == 0) {
        if (wave == 7) {
          const unsigned o1 = OFF_CS1 + (unsigned)(j * 96) * 4, o4 = OFF_CSGU + (unsigned)(j * 7 * 32) * 4;
          const unsigned lq = lane < 24 ? l16 : 0u, lg = lane < 56 ? l16 : 0u;
          u32x4 q0 = ld_w(cx, lq, o1), q1 = ld_w(cx, lq, o1 + 8192), g0 = ld_w(cx, lg, o4), g1 = ld_w(cx, lg, o4 + 16384);
          if (lane < 24) *(f32x4*)(smem + LDS_TAB + 2048 + l16) = __builtin_bit_cast(f32x4, q0) + __builtin_bit_cast(f32x4, q1);
          if (lane < 56) *(f32x4*)(smem + LDS_TAB + 1024 + l16) = __builtin_bit_cast(f32x4, g0) + __builtin_bit_cast(f32x4, g1);
        } else {
          WAIT_VM(14);
        }
        BAR_LDS();
        SB();
      }
      STAMP(2);
      f32x4 acc[6], gram = {0, 0, 0, 0};
#pragma unroll
      for (int c = 0; c < 6; ++c) acc[c] = f32x4{0, 0, 0, 0};
      u32x4 wb[2][6];
#pragma unroll
      for (int c = 0; c < 6; ++c) wb[0][c] = *(const u32x4*)(RW + c * 1024 + l16);
#pragma unroll
      for (int s = 0; s < KS; ++s) {
        if (s + 1 < KS) {
#pragma unroll
          for (int c = 0; c < 6; ++c) {
            const int ti = (s + 1) * 6 + c;
            wb[(s + 1) & 1][c] = *(const u32x4*)((ti < 56 ? RW + ti * 1024 : smem + LDS_W1B + (ti - 56) * 1024) + l16);
          }
        }
        gram = mfma16(a[s], a[s], gram);
#pragma unroll
        for (int c = 0; c < 6; ++c) acc[c] = mfma16(wb[s & 1][c], a[s], acc[c]);
        SB();
      }
      STAMP(3);
      const f32x2 st = stat_finish(red_add_g(g == 3 ? acc[1][0] : 0.f), gram, tok, g);
      const float mi = st[0] * st[1];
      unsigned pk[6][2];
#pragma unroll
      for (int c = 0; c < 6; ++c) {
        const f32x4 csum = *(const f32x4*)(smem + LDS_TAB + 2048 + c * 64 + g * 16);
        float v[4];
#pragma unroll
        for (int r = 0; r < 4; ++r) v[r] = fmaf(acc[c][r], st[1], -mi * csum[r]);
        if (c == 1 && g == 3) v[0] = 0.0f;
        if (c == 5 && g == 3) v[0] = 1.0f;
        pk[c][0] = pk_bf16(v[0], v[1]);
        pk[c][1] = pk_bf16(v[2], v[3]);
      }
      qf = u32x4{pk[0][0], pk[0][1], pk[1][0], pk[1][1]};
      u32x4 kf = {pk[2][0], pk[2][1], pk[3][0], pk[3][1]};
      u32x4 vf = (g & 1) ? u32x4{pk[5][0], pk[5][1], pk[4][0], pk[4][1]} : u32x4{pk[4][0], pk[4][1], pk[5][0], pk[5][1]};
      const unsigned vslot = (unsigned)(g * 256 + ((tok * 16 + (g >> 1) * 128) & 255));
      st_sc1(cx, l16, OFF_K + L * KV_BYTES + (unsigned)(j * 128 + tfw) * 1024, kf);
      st_sc1(cx, vslot, OFF_V + L * KV_BYTES + (unsigned)(j * 128 + tfw) * 1024, vf);
      *(u32x4*)(RA + (16 + wave) * 1024 + l16) = kf;
      *(u32x4*)(RA + (24 + 16 + wave) * 1024 + vslot) = vf;
      if (p < 2) {
        u32x4 z = {0, 0, 0, 0};
        asm volatile("" : "+v"(z));
#pragma unroll
        for (int i = 0; i < 2; ++i) {
          const int ti = wave * 2 + i;
          if (p * 8 - 16 + ti < 0) {
            *(u32x4*)(RA + ti * 1024 + l16) = z;
            *(u32x4*)(RA + (24 + ti) * 1024 + l16) = z;
          }
        }
      }
      WAIT_VM(0);
      BAR_LDS();
    }
    STAMP(4);
    if (tid == 448) {
      signal_add(cx, kvf_off(L, p, j));
      if (p >= 1) poll_ge(cx, kvf_off(L, p - 1, j), 1);
      if (p >= 2) poll_ge(cx, kvf_off(L, p - 2, j), 1);
    }
    const int Fi = j * 7 + (wave < 7 ? wave : 0);
    constexpr int W4A = 3, W4E = 10;
    u32x4 w4[KS][2];
    if (wave < 7) {
      if (projwg) {
#pragma unroll
        for (int i = 0; i < 8; ++i) dma<0>(cx, l16, OFF_WO + (unsigned)(nblk * 56 + wave * 8 + i) * 1024, RW + (wave * 8 + i) * 1024);
      }
#pragma unroll
      for (int s = 0; s < W4A; ++s) {
        w4[s][0] = ld_w(cx, l16, OFF_WGU + (unsigned)((Fi * KS + s) * 2) * 1024);
        w4[s][1] = ld_w(cx, l16, OFF_WGU + (unsigned)((Fi * KS + s) * 2 + 1) * 1024);
      }
    } else {
#pragma unroll
      for (int s = 0; s < W4A; ++s) { w4[s][0] = u32x4{0, 0, 0, 0}; w4[s][1] = u32x4{0, 0, 0, 0}; }
    }
    BAR();
#pragma unroll
    for (int i = 0; i < 2; ++i) {
      const int ti = wave * 2 + i, T = p * 8 - 16 + ti;
      if (T >= 0) {
        dma<16>(cx, l16, OFF_K + L * KV_BYTES + (unsigned)(j * 128 + T) * 1024, RA + ti * 1024);
        dma<16>(cx, l16, OFF_V + L * KV_BYTES + (unsigned)(j * 128 + T) * 1024, RA + (24 + ti) * 1024);
      }
    }
    WAIT_VM(0);
    BAR();
    STAMP(5);
    attn_body(cx, RA, qf, wave, lane, p, OFF_O + L * XT_BYTES + (unsigned)(tfw * KS) * 1024, j);
    STAMP(6);
    WAIT_VM(0);
    BAR();
    STAMP(7);
    if (tid == 448) {
      signal_add(cx, cnt_off(L, 0, p, 0));
      if (projwg) poll_ge(cx, cnt_off(L, 0, p, 0), 16);
    }
    if (wave < 7) {
#pragma unroll
      for (int s = W4A; s < W4E; ++s) {
        w4[s][0] = ld_w(cx, l16, OFF_WGU + (unsigned)((Fi * KS + s) * 2) * 1024);
        w4[s][1] = ld_w(cx, l16, OFF_WGU + (unsigned)((Fi * KS + s) * 2 + 1) * 1024);
      }
    } else {
#pragma unroll
      for (int s = W4A; s < W4E; ++s) { w4[s][0] = u32x4{0, 0, 0, 0}; w4[s][1] = u32x4{0, 0, 0, 0}; }
    }
    SB();
    if (projwg) {
      const int t3 = wave & 3, kh = wave >> 2;
      const int tf3 = p * 8 + qq * 4 + t3;
      const char* wlp = RW + kh * 28 * 1024 + l16;
      u32x4 wf[3][4];
#pragma unroll
      for (int i = 0; i < 2; ++i)
#pragma unroll
        for (int jj = 0; jj < 4; ++jj) wf[i][jj] = *(const u32x4*)(wlp + (i * 4 + jj) * 1024);
      u32x2 rs[2];
#pragma unroll
      for (int q = 0; q < 2; ++q) {
        const int jj = kh * 2 + q;
        const unsigned so = L ? (unsigned)(tf3 * KS + 2 * nblk + kh) * 1024 + q * 512 : (unsigned)tf3 * 14336u + nblk * 128 + jj * 32;
        rs[q] = __builtin_amdgcn_raw_buffer_load_b64(rsx, L ? r8t : r8r, xbase + so, 16);
      }
      BAR();
      STAMP(8);
      u32x4 a3[7];
#pragma unroll
      for (int i = 0; i < 7; ++i) a3[i] = ld_sc1(cx, l16, OFF_O + L * XT_BYTES + (unsigned)(tf3 * KS + kh * 7 + i) * 1024);
      SB();
      f32x4 acc[4];
#pragma unroll
      for (int jj = 0; jj < 4; ++jj) acc[jj] = f32x4{0, 0, 0, 0};
#pragma unroll
      for (int i = 0; i < 7; ++i) {
        if (i + 2 < 7) {
#pragma unroll
          for (int jj = 0; jj < 4; ++jj) wf[(i + 2) % 3][jj] = *(const u32x4*)(wlp + ((i + 2) * 4 + jj) * 1024);
        }
#pragma unroll
        for (int jj = 0; jj < 4; ++jj) acc[jj] = mfma16(wf[i % 3][jj], a3[i], acc[jj]);
        SB();
      }
      char* pp3 = smem + LDS_P3 + l16;
      *(f32x4*)(pp3 + (wave * 2 + 0) * 1024) = kh ? acc[0] : acc[2];
      *(f32x4*)(pp3 + (wave * 2 + 1) * 1024) = kh ? acc[1] : acc[3];
      BAR_LDS();
#pragma unroll
      for (int q = 0; q < 2; ++q) {
        f32x4 y = (kh ? acc[2 + q] : acc[q]) + *(const f32x4*)(pp3 + ((wave ^ 4) * 2 + q) * 1024);
        st_sc1_b64(cx, r8t, OFF_X1 + L * XT_BYTES + (unsigned)(tf3 * KS + 2 * nblk + kh) * 1024 + q * 512, add_res(y, rs[q]));
      }
      WAIT_VM(0);
    }
    BAR();
    STAMP(9);
    u32x4 w5[8][4];
    const unsigned w5base = OFF_WD + (unsigned)((nblk * 56 + wave * 8) * 4) * 1024;
    if (tid == 448) {
      if (projwg) signal_add(cx, cnt_off(L, 1, p, 0));
      poll_ge(cx, cnt_off(L, 1, p, 0), 14);
    }
    if (wave < 7) {
#pragma unroll
      for (int s = W4E; s < KS; ++s) {
        w4[s][0] = ld_w(cx, l16, OFF_WGU + (unsigned)((Fi * KS + s) * 2) * 1024);
        w4[s][1] = ld_w(cx, l16, OFF_WGU + (unsigned)((Fi * KS + s) * 2 + 1) * 1024);
      }
#pragma unroll
      for (int i = 0; i < 6; ++i) w5[i >> 2][i & 3] = ld_w(cx, l16, w5base + i * 1024);
    } else {
#pragma unroll
      for (int s = W4E; s < KS; ++s) { w4[s][0] = u32x4{0, 0, 0, 0}; w4[s][1] = u32x4{0, 0, 0, 0}; }
#pragma unroll
      for (int i = 0; i < 6; ++i) w5[i >> 2][i & 3] = u32x4{0, 0, 0, 0};
    }
    BAR();
    STAMP(10);
#pragma unroll
    for (int i = 0; i < 7; ++i) dma<16>(cx, l16, OFF_X1 + L * XT_BYTES + (unsigned)(p * 8 * KS + wave * 7 + i) * 1024, RA + (wave * 7 + i) * 1024);
    SB();
    asm volatile("" ::: "memory");
#pragma unroll
    for (int i = 0; i < 7; ++i) dma<16>(cx, l16, OFF_X1 + L * XT_BYTES + (unsigned)((p * 8 + 4) * KS + wave * 7 + i) * 1024, RW + (wave * 7 + i) * 1024);
    WAIT_VM(7);
    BAR();
    STAMP(11);
    const int tf5 = p * 8 + qq * 2;
    const int ft = wave >> 2, fj = wave & 3;
    const unsigned fso = (unsigned)(2 * nblk + (fj >> 1)) * 1024 + (fj & 1) * 512;
    u32x4 hb[8][2];
    bool hb_issued = false;
    if (wave < 7) {
#pragma unroll
      for (int hh = 0; hh < 2; ++hh) {
        const char* xt = (hh == 0 ? RA : RW) + l16;
        f32x4 acc[4][2];
#pragma unroll
        for (int t = 0; t < 4; ++t) { acc[t][0] = f32x4{0, 0, 0, 0}; acc[t][1] = f32x4{0, 0, 0, 0}; }
        u32x4 xb[3][4];
        auto rd = [&](int s) {
#pragma unroll
          for (int t = 0; t < 4; ++t) xb[s % 3][t] = *(const u32x4*)(xt + (t * KS + s) * 1024);
        };
        rd(0); rd(1);
        SB();
#pragma unroll
        for (int s = 0; s < KS; ++s) {
          if (s + 2 < KS) rd(s + 2);
#pragma unroll
          for (int t = 0; t < 4; ++t) {
            acc[t][0] = mfma16(w4[s][0], xb[s % 3][t], acc[t][0]);
            acc[t][1] = mfma16(w4[s][1], xb[s % 3][t], acc[t][1]);
          }
          SB();
          if (hh == 0 && s == 3) { WAIT_VM(0); ev_post(smem, E_X1B, lane); SB(); }
          if (hh == 0 && s >= 12) { w5[(s - 6) >> 2][(s - 6) & 3] = ld_w(cx, l16, w5base + (s - 6) * 1024); SB(); }
          if (hh == 1 && s < 12) {
#pragma unroll
            for (int q = 0; q < 2; ++q) { const int idx = 8 + s * 2 + q; w5[idx >> 2][idx & 3] = ld_w(cx, l16, w5base + idx * 1024); }
            SB();
          }
          if (hh == 1 && s == 2) {
            WAIT_VM(6);
            if (ev_arrive(smem, E_HD0, lane) == 7 * M1 - 1) { if (lane == 0) signal_add(cx, cnt_off(L, 2, p, 0)); }
            SB();
          }
        }
        STAMP(12 + hh * 2);
        ev_post(smem, E_T4A + hh, lane);
        ev_wait(smem, E_ST0 + hh, M1);
        const f32x4 cg = *(const f32x4*)(smem + LDS_TAB + 1024 + wave * 128 + g * 16);
        const f32x4 cu = *(const f32x4*)(smem + LDS_TAB + 1024 + wave * 128 + 64 + g * 16);
        unsigned hv[4][2];
#pragma unroll
        for (int t = 0; t < 4; ++t) {
          const f32x2 s2 = *(const f32x2*)(smem + LDS_TAB + ((hh * 4 + t) * 16 + tok) * 8);
          float gv[4], uv[4];
          float nmi = -s2[0], inv = s2[1];
#pragma unroll
          for (int r = 0; r < 4; ++r) {
            float tg = nmi * cg[r], tu = nmi * cu[r];
            asm volatile("" : "+v"(tg), "+v"(tu));
            gv[r] = fmaf(acc[t][0][r], inv, tg);
            uv[r] = fmaf(acc[t][1][r], inv, tu);
            asm volatile("" : "+v"(gv[r]), "+v"(uv[r]));
          }
          unsigned g0 = pk_bf16(gv[0], gv[1]), g1 = pk_bf16(gv[2], gv[3]);
          unsigned u0 = pk_bf16(uv[0], uv[1]), u1 = pk_bf16(uv[2], uv[3]);
          hv[t][0] = pk_bf16(bf_lo(g0) * bf_lo(u0), bf_hi(g0) * bf_hi(u0));
          hv[t][1] = pk_bf16(bf_lo(g1) * bf_lo(u1), bf_hi(g1) * bf_hi(u1));
        }
        const unsigned hlane = (unsigned)((g & 1) * (KSF * 1024) + ((Fi & 1) * 2 + (g >> 1)) * 256 + tok * 16);
#pragma unroll
        for (int u = 0; u < 2; ++u) {
          auto s0 = __builtin_amdgcn_permlane16_swap(hv[2 * u][0], hv[2 * u + 1][0], false, false);
          auto s1 = __builtin_amdgcn_permlane16_swap(hv[2 * u][1], hv[2 * u + 1][1], false, false);
          u32x4 o = {s0[0], s1[0], s0[1], s1[1]};
          st_sc1(cx, hlane, OFF_H + L * H_BYTES + (unsigned)((p * 8 + hh * 4 + 2 * u) * KSF + (Fi >> 1)) * 1024, o);
        }
        SB();
        if (hh == 0) { ev_wait(smem, E_X1B, 8 * M1); STAMP(13); }
      }
      hb_issued = projwg && ev_peek(smem, E_HR0) >= M1;
      asm volatile("" ::: "memory");
      if (hb_issued) {
        const unsigned hbase = OFF_H + L * H_BYTES + (unsigned)(tf5 * KSF + wave * 8) * 1024;
#pragma unroll
        for (int i = 0; i < 8; ++i)
#pragma unroll
          for (int t = 0; t < 2; ++t) hb[i][t] = ld_sc1(cx, l16, hbase + (unsigned)(t * KSF + i) * 1024);
        SB();
        WAIT_VM(16);
      } else {
#pragma unroll
        for (int i = 0; i < 8; ++i) { hb[i][0] = u32x4{0, 0, 0, 0}; hb[i][1] = u32x4{0, 0, 0, 0}; }
        WAIT_VM(0);
      }
      if (ev_arrive(smem, E_HD1, lane) == 7 * M1 - 1) { if (lane == 0) signal_add(cx, cnt_off(L, 2, p, 1)); }
      STAMP(15);
    } else {
#pragma unroll
      for (int i = 6; i < 32; ++i) w5[i >> 2][i & 3] = u32x4{0, 0, 0, 0};
#pragma unroll
      for (int i = 0; i < 8; ++i) { hb[i][0] = u32x4{0, 0, 0, 0}; hb[i][1] = u32x4{0, 0, 0, 0}; }
      __builtin_amdgcn_s_setprio(3);
      const u32x4 ones = {0x3f803f80u, 0x3f803f80u, 0x3f803f80u, 0x3f803f80u};
#pragma unroll
      for (int hh = 0; hh < 2; ++hh) {
        if (hh == 1) { WAIT_VM(0); ev_post(smem, E_X1B, lane); ev_wait(smem, E_X1B, 8 * M1); }
        const char* xt = (hh == 0 ? RA : RW) + l16;
        f32x4 sm[4], gr[4];
#pragma unroll
        for (int t = 0; t < 4; ++t) { sm[t] = f32x4{0, 0, 0, 0}; gr[t] = f32x4{0, 0, 0, 0}; }
        u32x4 xb[3][4];
        auto rd = [&](int s) {
#pragma unroll
          for (int t = 0; t < 4; ++t) xb[s % 3][t] = *(const u32x4*)(xt + (t * KS + s) * 1024);
        };
        rd(0); rd(1);
        SB();
#pragma unroll
        for (int s = 0; s < KS; ++s) {
          if (s + 2 < KS) rd(s + 2);
#pragma unroll
          for (int t = 0; t < 4; ++t) {
            sm[t] = mfma16(ones, xb[s % 3][t], sm[t]);
            gr[t] = mfma16(xb[s % 3][t], xb[s % 3][t], gr[t]);
          }
          SB();
        }
#pragma unroll
        for (int t = 0; t < 4; ++t) {
          const f32x2 st = stat_finish(sm[t][0], gr[t], tok, g);
          if (g == 0) *(f32x2*)(smem + LDS_TAB + ((hh * 4 + t) * 16 + tok) * 8) = f32x2{st[0] * st[1], st[1]};
        }
        ev_post(smem, E_ST0 + hh, lane);
        STAMP(12 + hh);
      }
      __builtin_amdgcn_s_setprio(0);
      if (L == 0) {
        ev_wait(smem, E_T4A, 7 * M1);
#pragma unroll 1
        for (int i = 0; i < 84; i += 4) {
#pragma unroll
          for (int q = 0; q < 4; ++q)
            dma<0>(cx, l16, OFF_W1 + (unsigned)(j * 84 + i + q) * 1024, i < 56 ? RA + (i + q) * 1024 : smem + LDS_W1B + (i - 56 + q) * 1024);
        }
        SB();
      }
      STAMP(14);
      if (projwg) {
        if (lane == 0) poll_ge(cx, cnt_off(L, 2, p, 0), 16);
        ev_post(smem, E_HR0, lane);
        STAMP(15);
        if (lane == 0) poll_ge(cx, cnt_off(L, 2, p, 1), 16);
        ev_post(smem, E_HR1, lane);
        STAMP(16);
      }
    }
    if (projwg) {
      u32x2 r5[2];
#pragma unroll
      for (int hh = 0; hh < 2; ++hh)
        r5[hh] = __builtin_amdgcn_raw_buffer_load_b64(cx.rs, r8t, OFF_X1 + L * XT_BYTES + (unsigned)((tf5 + hh * 4 + ft) * KS) * 1024 + fso, 16);
#pragma unroll
      for (int hh = 0; hh < 2; ++hh) {
        if (wave < 7) {
          if (!hb_issued) {
            ev_wait(smem, E_HR0 + hh, M1);
            const unsigned hbase = OFF_H + L * H_BYTES + (unsigned)((tf5 + hh * 4) * KSF + wave * 8) * 1024;
#pragma unroll
            for (int i = 0; i < 8; ++i)
#pragma unroll
              for (int t = 0; t < 2; ++t) hb[i][t] = ld_sc1(cx, l16, hbase + (unsigned)(t * KSF + i) * 1024);
          }
          STAMP(16 + hh * 3);
          SB();
          f32x4 acc[4][2];
#pragma unroll
          for (int jj = 0; jj < 4; ++jj) { acc[jj][0] = f32x4{0, 0, 0, 0}; acc[jj][1] = f32x4{0, 0, 0, 0}; }
#pragma unroll
          for (int i = 0; i < 8; ++i)
#pragma unroll
            for (int jj = 0; jj < 4; ++jj)
#pragma unroll
              for (int t = 0; t < 2; ++t) acc[jj][t] = mfma16(w5[i][jj], hb[i][t], acc[jj][t]);
          STAMP(17 + hh * 3);
          if (hh == 0) { ev_wait(smem, E_T4D, 7 * M1); ev_wait(smem, E_ST1, M1); } else ev_wait(smem, E_P5F, 8 * M1);
#pragma unroll
          for (int t = 0; t < 2; ++t)
#pragma unroll
            for (int jj = 0; jj < 4; ++jj) *(f32x4*)(RW + (wave * 8 + t * 4 + jj) * 1024 + l16) = acc[jj][t];
          ev_post(smem, E_P5P0 + hh, lane);
          if (hh == 0) {
            hb_issued = ev_peek(smem, E_HR1) >= M1;
            asm volatile("" ::: "memory");
            if (hb_issued) {
              const unsigned hbase = OFF_H + L * H_BYTES + (unsigned)((tf5 + 4) * KSF + wave * 8) * 1024;
#pragma unroll
              for (int i = 0; i < 8; ++i)
#pragma unroll
                for (int t = 0; t < 2; ++t) hb[i][t] = ld_sc1(cx, l16, hbase + (unsigned)(t * KSF + i) * 1024);
            }
            SB();
          }
        }
        ev_wait(smem, E_P5P0 + hh, 7 * M1);
        STAMP(18 + hh * 3);
        f32x4 y = *(const f32x4*)(RW + wave * 1024 + l16);
#pragma unroll
        for (int sw = 1; sw < 7; ++sw) y += *(const f32x4*)(RW + (sw * 8 + wave) * 1024 + l16);
        if (hh == 0) ev_post(smem, E_P5F, lane);
        const u32x2 o = add_res(y, r5[hh]);
        const int tf = tf5 + hh * 4 + ft;
        if (L == 0) st_sc1_b64(cx, r8t, OFF_X + XT_BYTES + (unsigned)(tf * KS) * 1024 + fso, o);
        else __builtin_amdgcn_raw_buffer_store_b64(o, ro, r8r, (unsigned)tf * 14336u + nblk * 128 + fj * 32, 0);
      }
      if (L == 0) {
        WAIT_VM(0);
        if (ev_arrive(smem, E_X2D, lane) == 7) { if (lane == 0) signal_add(cx, cnt_off(L, 3, p, 0)); }
      }
      STAMP(22);
    }
    WAIT_VM(0);
  }
}

extern "C" void kernel_launch(void* const* d_in, const int* in_sizes, int n_in, void* d_out, int out_size, void* d_ws,
                              size_t ws_size, hipStream_t stream) {
  const uint16_t* x = (const uint16_t*)d_in[0];
  const uint16_t* wqkv = (const uint16_t*)d_in[1];
  const uint16_t* wo = (const uint16_t*)d_in[2];
  const uint16_t* wg = (const uint16_t*)d_in[4];
  const uint16_t* wu = (const uint16_t*)d_in[5];
  const uint16_t* wd = (const uint16_t*)d_in[6];
  char* ws = (char*)d_ws;
  constexpr int LP = 224 * 192;
  (void)hipFuncSetAttribute((const void*)k_main, hipFuncAttributeMaxDynamicSharedMemorySize, LDS_TOTAL);
  k_prep<<<215, 512, LP, stream>>>(x, wqkv, wo, wg, wu, wd, ws);
  k_main<<<256, 512, LDS_TOTAL, stream>>>(ws, (const char*)x, (char*)d_out);
}
```

```cpp
#include <hip/hip_runtime.h>
#include <hip/hip_bf16.h>
#include <stdint.h>

typedef __attribute__((ext_vector_type(8))) short s16x8;
typedef __attribute__((ext_vector_type(4))) short s16x4;
typedef __attribute__((ext_vector_type(4))) float f32x4;
typedef __attribute__((ext_vector_type(2))) float f32x2;
typedef __attribute__((ext_vector_type(2))) __bf16 bf16x2;
typedef __attribute__((ext_vector_type(4))) unsigned u32x4;
typedef __attribute__((ext_vector_type(2))) unsigned u32x2;
#define LAS __attribute__((address_space(3)))
typedef __attribute__((address_space(1))) unsigned gu32;

static constexpr int S_ = 2048, D_ = 448, F_ = 1792, NH = 16, DH = 28;
static constexpr int KS = 14, KSF = 56;

static constexpr unsigned OFF_CNT = 0;
static constexpr unsigned OFF_TMO = 196608;
static constexpr unsigned FLAG_BYTES = 196608 + 1024;
static constexpr unsigned OFF_W1 = 262144;
static constexpr unsigned OFF_CS1 = OFF_W1 + 16 * 14 * 6 * 1024;
static constexpr unsigned OFF_WO = OFF_CS1 + 16384;
static constexpr unsigned OFF_WGU = OFF_WO + 7 * 14 * 4 * 1024;
static constexpr unsigned OFF_CSGU = OFF_WGU + 112 * 14 * 2 * 1024;
static constexpr unsigned OFF_WD = OFF_CSGU + 32768;
static constexpr unsigned XT_BYTES = 128 * 14 * 1024;
static constexpr unsigned OFF_X = OFF_WD + 7 * 56 * 4 * 1024;
static constexpr unsigned OFF_X1 = OFF_X + 2 * XT_BYTES;
static constexpr unsigned OFF_O = OFF_X1 + 2 * XT_BYTES;
static constexpr unsigned KV_BYTES = 16 * 128 * 1024;
static constexpr unsigned OFF_K = OFF_O + 2 * XT_BYTES;
static constexpr unsigned OFF_V = OFF_K + 2 * KV_BYTES;
static constexpr unsigned H_BYTES = 128 * 56 * 1024;
static constexpr unsigned OFF_H = OFF_V + 2 * KV_BYTES;
static constexpr unsigned ST_BYTES = 2048 * 64;
static constexpr unsigned OFF_STX = OFF_H + 2 * H_BYTES;
static constexpr unsigned OFF_STX1 = OFF_STX + 2 * ST_BYTES;

__device__ __forceinline__ float bf_lo(unsigned u) { return __uint_as_float(u << 16); }
__device__ __forceinline__ float bf_hi(unsigned u) { return __uint_as_float(u & 0xffff0000u); }
__device__ __forceinline__ unsigned pk_bf16(float a, float b) {
  f32x2 f = {a, b};
  bf16x2 r = __builtin_convertvector(f, bf16x2);
  return __builtin_bit_cast(unsigned, r);
}
__device__ __forceinline__ float rnd_bf16(float a) { return bf_lo(pk_bf16(a, 0.f)); }
__device__ __forceinline__ float red_add_g(float v) {
  auto p = __builtin_amdgcn_permlane16_swap(__float_as_uint(v), __float_as_uint(v), false, false);
  v = __uint_as_float(p[0]) + __uint_as_float(p[1]);
  auto q = __builtin_amdgcn_permlane32_swap(__float_as_uint(v), __float_as_uint(v), false, false);
  return __uint_as_float(q[0]) + __uint_as_float(q[1]);
}
__device__ __forceinline__ float red_max_g(float v) {
  auto p = __builtin_amdgcn_permlane16_swap(__float_as_uint(v), __float_as_uint(v), false, false);
  v = fmaxf(__uint_as_float(p[0]), __uint_as_float(p[1]));
  auto q = __builtin_amdgcn_permlane32_swap(__float_as_uint(v), __float_as_uint(v), false, false);
  return fmaxf(__uint_as_float(q[0]), __uint_as_float(q[1]));
}
__device__ __forceinline__ f32x4 mfma16(u32x4 a, u32x4 b, f32x4 c) {
  return __builtin_amdgcn_mfma_f32_16x16x32_bf16(__builtin_bit_cast(s16x8, a), __builtin_bit_cast(s16x8, b), c, 0, 0, 0);
}
__device__ __forceinline__ u32x4 tr_frag(const char* p0, const char* p1) {
  s16x4 lo = __builtin_amdgcn_ds_read_tr16_b64_v4i16((LAS s16x4*)p0);
  s16x4 hi = __builtin_amdgcn_ds_read_tr16_b64_v4i16((LAS s16x4*)p1);
  u32x2 l2 = __builtin_bit_cast(u32x2, lo), h2 = __builtin_bit_cast(u32x2, hi);
  return u32x4{l2[0], l2[1], h2[0], h2[1]};
}
#define SB() __builtin_amdgcn_sched_barrier(0)
#define STAMP(i)
#define WAIT_VM(n) asm volatile("s_waitcnt vmcnt(" #n ")" ::: "memory")
#define BAR_LDS() do { asm volatile("s_waitcnt lgkmcnt(0)" ::: "memory"); __builtin_amdgcn_s_barrier(); asm volatile("" ::: "memory"); } while (0)

__device__ __forceinline__ f32x2 stat_finish(float sum, f32x4 gram, int tok, int g) {
  float d = (tok & 2) ? ((tok & 1) ? gram[3] : gram[2]) : ((tok & 1) ? gram[1] : gram[0]);
  d = (g == (tok >> 2)) ? d : 0.f;
  const float sq = red_add_g(d);
  const float mean = sum * (1.0f / D_);
  const float var = fmaxf(fmaf(-mean, mean, sq * (1.0f / D_)), 0.f);
  const float mb = rnd_bf16(mean);
  const float den = rnd_bf16(__builtin_amdgcn_sqrtf(rnd_bf16(rnd_bf16(var) + 1e-5f)));
  return f32x2{mb, __builtin_amdgcn_rcpf(den)};
}

__device__ __forceinline__ void tile_block(const uint16_t* __restrict__ src, int ld, int col0, const int (&rows)[8], char* dst,
                                           int jjstride, int lane, u32x4 (&o)[4], bool consec = false) {
  const int i = lane & 15;
  u32x2 v[8];
#pragma unroll
  for (int e = 0; e < 8; ++e) v[e] = *(const u32x2*)(src + (size_t)rows[e] * ld + col0 + 4 * i);
#pragma unroll
  for (int jj = 0; jj < 4; ++jj) {
#pragma unroll
    for (int d = 0; d < 4; ++d)
      o[jj][d] = __builtin_amdgcn_perm(v[2 * d + 1][jj >> 1], v[2 * d][jj >> 1], (jj & 1) ? 0x07060302u : 0x05040100u);
    if (consec) *(u32x4*)(dst + (size_t)(i >> 2) * jjstride + ((lane >> 4) * 16 + 4 * (i & 3) + jj) * 16) = o[jj];
    else *(u32x4*)(dst + (size_t)jj * jjstride + lane * 16) = o[jj];
  }
}

__global__ __launch_bounds__(512, 2) void k_prep(const uint16_t* __restrict__ x, const uint16_t* __restrict__ wqkv,
                                                 const uint16_t* __restrict__ wo, const uint16_t* __restrict__ wg,
                                                 const uint16_t* __restrict__ wu, const uint16_t* __restrict__ wd,
                                                 char* __restrict__ ws) {
  extern __shared__ __attribute__((aligned(16))) char smem[];
  const int tid = threadIdx.x, lane = tid & 63, wave = __builtin_amdgcn_readfirstlane(tid >> 6);
  const int b = blockIdx.x;
  const int g = lane >> 4;
  const u32x4 ones = {0x3f803f80u, 0x3f803f80u, 0x3f803f80u, 0x3f803f80u};
  if (b < 112) {
    const int kh = b & 1, which = (b >> 1) & 1, blk = b >> 2;
    if (wave < 7) {
      const int s = kh * 7 + wave;
      int rows[8];
#pragma unroll
      for (int e = 0; e < 8; ++e) rows[e] = 32 * s + 8 * g + e;
      u32x4 o[4];
      tile_block(which ? wu : wg, F_, blk * 64, rows, ws + OFF_WGU + (size_t)(((blk * 4) * 14 + s) * 2 + which) * 1024,
                 14 * 2 * 1024, lane, o);
#pragma unroll
      for (int jj = 0; jj < 4; ++jj) {
        f32x4 cs = mfma16(o[jj], ones, f32x4{0, 0, 0, 0});
        if ((lane & 15) == 0) *(f32x4*)(smem + ((wave * 4 + jj) * 4 + g) * 16) = cs;
      }
    }
    __syncthreads();
    if (tid < 64) {
      float s = 0.f;
#pragma unroll
      for (int w = 0; w < 7; ++w) s += *(const float*)(smem + (w * 64 + tid) * 4);
      const int jj = tid >> 4, row = tid & 15;
      *(float*)(ws + OFF_CSGU + (size_t)kh * 16384 + (size_t)(((blk * 4 + jj) * 2 + which) * 16 + row) * 4) = s;
    }
  } else if (b < 126) {
    const int t = b - 112, nblk = t >> 1;
    if (wave < 7) {
      const int s = (t & 1) * 7 + wave;
      int rows[8];
#pragma unroll
      for (int e = 0; e < 8; ++e) rows[e] = 32 * s + 8 * g + e;
      u32x4 o[4];
      tile_block(wo, D_, nblk * 64, rows, ws + OFF_WO + (size_t)((nblk * 14 + s) * 4) * 1024, 1024, lane, o, true);
    }
  } else if (b < 182) {
    const int t = b - 126, nblk = t >> 3;
    if (wave < 7) {
      const int s = (t & 7) * 7 + wave;
      const int Fi = 2 * s + (g >> 1);
      int rows[8];
#pragma unroll
      for (int e = 0; e < 8; ++e) rows[e] = 64 * (Fi >> 2) + 16 * (2 * (g & 1) + (e >> 2)) + 4 * (e & 3) + (Fi & 3);
      u32x4 o[4];
      tile_block(wd, D_, nblk * 64, rows, ws + OFF_WD + (size_t)((nblk * 56 + s) * 4) * 1024, 1024, lane, o, true);
    }
  } else if (b < 214) {
    const int head = (b - 182) >> 1, kh = (b - 182) & 1;
    u32x2 v[11];
#pragma unroll
    for (int it = 0; it < 11; ++it) {
      const int c = it * 512 + tid;
      const int row = c / 24, grp = c - row * 24;
      const int part = grp >> 3, gi = grp & 7;
      v[it] = u32x2{grp == 15 ? 0x00003f80u : 0u, 0u};
      if (gi < 7 && row < 224) v[it] = *(const u32x2*)(wqkv + (size_t)(kh * 224 + row) * (3 * D_) + part * D_ + head * DH + gi * 4);
    }
#pragma unroll
    for (int it = 0; it < 11; ++it) {
      const int c = it * 512 + tid;
      const int row = c / 24, grp = c - row * 24;
      int pp = (grp >> 2) + ((row >> 3) & 1);
      if (pp >= 6) pp -= 6;
      if (row < 224) *(u32x2*)(smem + row * 192 + pp * 32 + (grp & 3) * 8) = v[it];
    }
    __syncthreads();
    if (wave < 6) {
      const int c = wave;
      const int q4 = (lane & 15) >> 2, p4 = lane & 3;
      int pp = c + (g & 1);
      if (pp >= 6) pp -= 6;
      const char* base = smem + (8 * g + q4) * 192 + pp * 32 + p4 * 8;
      f32x4 cs = {0, 0, 0, 0};
#pragma unroll
      for (int s = 0; s < 7; ++s) {
        u32x4 wf = tr_frag(base + s * 32 * 192, base + s * 32 * 192 + 4 * 192);
        *(u32x4*)(ws + OFF_W1 + (size_t)((head * 14 + kh * 7 + s) * 6 + c) * 1024 + lane * 16) = wf;
        cs = mfma16(wf, ones, cs);
      }
      if ((lane & 15) == 0) *(f32x4*)(ws + OFF_CS1 + (size_t)kh * 8192 + (size_t)(head * 96 + c * 16 + 4 * g) * 4) = cs;
    }
  } else {
#pragma unroll 1
    for (unsigned o = ((b - 214) * 512 + tid) * 16; o < FLAG_BYTES; o += 8 * 512 * 16) *(u32x4*)(ws + o) = u32x4{0, 0, 0, 0};
    {
      unsigned acc = 0;
      for (unsigned o = OFF_X + tid * 65536u; o < OFF_STX; o += 512u * 65536u) acc += *(const volatile unsigned*)(ws + o);
      if (tid < 28) acc += *(const volatile unsigned*)((const char*)x + tid * 65536u);
      if (acc == 0x12345678u) *(unsigned*)(ws + OFF_STX) = acc;
    }
  }
}

#define MAPMODE 2
static constexpr int LDS_REG = 57344;
static constexpr int LDS_W1B = 114688;
static constexpr int LDS_P3 = 143360;
static constexpr int LDS_TAB = 159744;
static constexpr int LDS_EV = 162816;
static constexpr int LDS_TOTAL = 163840;

struct Ctx {
  __amdgpu_buffer_rsrc_t rs;
  char* ws;
};
__device__ __forceinline__ u32x4 ld_sc1(const Ctx& c, unsigned voff, unsigned soff) { return __builtin_amdgcn_raw_buffer_load_b128(c.rs, voff, soff, 16); }
__device__ __forceinline__ u32x4 ld_w(const Ctx& c, unsigned voff, unsigned soff) { return __builtin_amdgcn_raw_buffer_load_b128(c.rs, voff, soff, 0); }
__device__ __forceinline__ void st_sc1(const Ctx& c, unsigned voff, unsigned soff, u32x4 v) { __builtin_amdgcn_raw_buffer_store_b128(v, c.rs, voff, soff, 16); }
__device__ __forceinline__ void st_sc1_b64(const Ctx& c, unsigned voff, unsigned soff, u32x2 v) { __builtin_amdgcn_raw_buffer_store_b64(v, c.rs, voff, soff, 16); }
template <int AUX>
__device__ __forceinline__ void dma(const Ctx& c, unsigned voff, unsigned soff, char* lds_uniform) {
  __builtin_amdgcn_raw_ptr_buffer_load_lds(c.rs, (LAS void*)lds_uniform, 16, voff, soff, 0, AUX);
}
__device__ __forceinline__ unsigned flag_line(int L, int e, int p, int c) { return OFF_CNT + (unsigned)((((L * 6 + e) * 16 + p) * 16 + c) * 64); }
__device__ __forceinline__ void signal16(const Ctx& c, int L, int e, int p, int j, int lane) {
  if (lane < 16) __hip_atomic_store((gu32*)(c.ws + flag_line(L, e, p, lane) + j * 4), 1u, __ATOMIC_RELAXED, __HIP_MEMORY_SCOPE_AGENT);
}
__device__ __forceinline__ void poll16(const Ctx& c, unsigned line, unsigned need, int lane) {
  gu32* f = (gu32*)(c.ws + line + (lane & 15) * 4);
  gu32* tmo = (gu32*)(c.ws + OFF_TMO);
  const bool skip = ((need >> (lane & 15)) & 1u) == 0u;
  unsigned spins = 0;
  for (;;) {
    const unsigned v = __hip_atomic_load(f, __ATOMIC_RELAXED, __HIP_MEMORY_SCOPE_AGENT);
    if (__all(skip || v != 0u)) break;
    ++spins;
    if ((spins & 1023u) == 0u) {
      if (spins > (1u << 20) || __hip_atomic_load(tmo, __ATOMIC_RELAXED, __HIP_MEMORY_SCOPE_AGENT) != 0u) {
        __hip_atomic_store(tmo, 1u, __ATOMIC_RELAXED, __HIP_MEMORY_SCOPE_AGENT);
        break;
      }
    }
  }
  __builtin_amdgcn_fence(__ATOMIC_ACQUIRE, "wavefront");
}
enum { F_KV, F_O, F_X1, F_HA, F_HB, F_X2 };
static constexpr unsigned ALL16 = 0xffffu, PROJ14 = 0x7f7fu;

__device__ __forceinline__ float max3f(float a, float b, float c) { float r; asm("v_max3_f32 %0, %1, %2, %3" : "=v"(r) : "v"(a), "v"(b), "v"(c)); return r; }
template <typename PF>
__device__ __forceinline__ void attn_body(const Ctx& cx, const char* kv, u32x4 qf, int w, int lane, int p, unsigned ooff, int head, PF&& pf_) {
  const int tok = lane & 15, g = lane >> 4;
  const int t0 = w & ~1;
  const char* kl = kv + t0 * 1024 + lane * 16;
  f32x4 sc[18];
  {
    u32x4 kf[2][3];
#pragma unroll
    for (int q = 0; q < 3; ++q) kf[0][q] = *(const u32x4*)(kl + q * 1024);
#pragma unroll
    for (int gi = 0; gi < 6; ++gi) {
      if (gi + 1 < 6) {
#pragma unroll
        for (int q = 0; q < 3; ++q) kf[(gi + 1) & 1][q] = *(const u32x4*)(kl + ((gi + 1) * 3 + q) * 1024);
      }
#pragma unroll
      for (int q = 0; q < 3; ++q) sc[gi * 3 + q] = mfma16(kf[gi & 1][q], qf, f32x4{0, 0, 0, 0});
      if (gi == 1 || gi == 4) pf_(gi >> 2);
      SB();
    }
  }
  asm volatile("s_nop 15\n\ts_nop 15\n\ts_nop 15" ::: "memory");
  SB();
  const int dq = 16 * (16 + w) + tok - (16 * t0 + 4 * g);
  const float NEG = -INFINITY;
#pragma unroll
  for (int i = 0; i < 18; ++i) {
    if (i < 2 || i >= 16) {
#pragma unroll
      for (int r = 0; r < 4; ++r) sc[i][r] = ((unsigned)(dq - 16 * i - r) < 256u) ? sc[i][r] : NEG;
    }
  }
  if (p < 2) {
    const int nneg = 16 - 8 * p - t0;
#pragma unroll
    for (int i = 0; i < 16; ++i)
      if (i < nneg) sc[i] = f32x4{NEG, NEG, NEG, NEG};
  }
  float m0 = max3f(sc[0][0], sc[0][1], sc[0][2]), m1 = max3f(sc[0][3], sc[1][0], sc[1][1]), m2 = max3f(sc[1][2], sc[1][3], sc[1][3]);
#pragma unroll
  for (int i = 2; i < 17; i += 3) {
    m0 = max3f(m0, sc[i][0], sc[i][1]); m1 = max3f(m1, sc[i][2], sc[i][3]);
    m2 = max3f(m2, sc[i + 1][0], sc[i + 1][1]); m0 = max3f(m0, sc[i + 1][2], sc[i + 1][3]);
    m1 = max3f(m1, sc[i + 2][0], sc[i + 2][1]); m2 = max3f(m2, sc[i + 2][2], sc[i + 2][3]);
  }
  m0 = max3f(m0, sc[17][0], sc[17][1]); m1 = max3f(m1, sc[17][2], sc[17][3]);
  float m = max3f(m0, m1, m2);
  {
    auto pp = __builtin_amdgcn_permlane16_swap(__float_as_uint(m), __float_as_uint(m), false, false);
    m = max3f(__uint_as_float(pp[0]), __uint_as_float(pp[1]), __uint_as_float(pp[1]));
    auto qq = __builtin_amdgcn_permlane32_swap(__float_as_uint(m), __float_as_uint(m), false, false);
    m = max3f(__uint_as_float(qq[0]), __uint_as_float(qq[1]), __uint_as_float(qq[1]));
  }
  const float cs = 0.18898223650461362f * 1.4426950408889634f;
  const float mc = m * cs;
  const f32x2 cs2 = {cs, cs}, mc2 = {-mc, -mc};
  u32x4 pf[9];
#pragma unroll
  for (int i = 0; i < 18; ++i) {
    f32x2 x0 = __builtin_elementwise_fma(f32x2{sc[i][0], sc[i][1]}, cs2, mc2);
    f32x2 x1 = __builtin_elementwise_fma(f32x2{sc[i][2], sc[i][3]}, cs2, mc2);
    pf[i >> 1][(i & 1) * 2 + 0] = pk_bf16(__builtin_amdgcn_exp2f(x0[0]), __builtin_amdgcn_exp2f(x0[1]));
    pf[i >> 1][(i & 1) * 2 + 1] = pk_bf16(__builtin_amdgcn_exp2f(x1[0]), __builtin_amdgcn_exp2f(x1[1]));
    if ((i & 3) == 1 && i < 16) { SB(); pf_(2 + (i >> 2)); SB(); }
  }
  const int q4 = (lane & 15) >> 2, p4 = lane & 3;
  const char* vl = kv + 24 * 1024 + t0 * 1024;
  const int vb = p4 * 256 + (((4 * g + q4) * 16 + (p4 >> 1) * 128) & 255);
  const int vofs[2] = {vb + (p4 & 1) * 8, vb + ((p4 & 1) ^ 1) * 8};
  f32x4 o[2] = {f32x4{0, 0, 0, 0}, f32x4{0, 0, 0, 0}};
  SB();
  {
    u32x4 vf[3][2];
    auto rdv = [&](int bb) {
#pragma unroll
      for (int f = 0; f < 2; ++f) vf[bb % 3][f] = tr_frag(vl + vofs[f] + (2 * bb) * 1024, vl + vofs[f] + (2 * bb + 1) * 1024);
    };
    rdv(0); rdv(1);
#pragma unroll
    for (int bb = 0; bb < 9; ++bb) {
      if (bb + 2 < 9) rdv(bb + 2);
#pragma unroll
      for (int f = 0; f < 2; ++f) o[f] = mfma16(vf[bb % 3][f], pf[bb], o[f]);
      SB();
    }
  }
  const float lsum = __shfl(o[1][0], 48 + tok);
  const float inv = __builtin_amdgcn_rcpf(lsum);
#pragma unroll
  for (int f = 0; f < 2; ++f) {
    if (f == 0 || g < 3) {
      const int k0 = DH * head + 16 * f + 4 * g;
      u32x2 st = {pk_bf16(o[f][0] * inv, o[f][1] * inv), pk_bf16(o[f][2] * inv, o[f][3] * inv)};
      st_sc1_b64(cx, (unsigned)(((k0 >> 5) * 64 + ((k0 & 31) >> 3) * 16 + tok) * 16 + (k0 & 7) * 2), ooff, st);
    }
  }
}

enum { E_X1B, E_ST0, E_ST1, E_HD0, E_HD1, E_HR0, E_HR1, E_T4A, E_T4D, E_P5P0, E_P5P1, E_P5F, E_X2D, E_COUNT };
__device__ __forceinline__ unsigned ev_arrive(char* smem, int e, int lane) {
  asm volatile("s_waitcnt lgkmcnt(0)" ::: "memory");
  unsigned old = 0;
  if (lane == 0) old = __hip_atomic_fetch_add((LAS unsigned*)(smem + LDS_EV + e * 4), 1u, __ATOMIC_RELAXED, __HIP_MEMORY_SCOPE_WORKGROUP);
  return __builtin_amdgcn_readfirstlane(old);
}
__device__ __forceinline__ void ev_post(char* smem, int e, int lane) {
  asm volatile("s_waitcnt lgkmcnt(0)" ::: "memory");
  if (lane == 0) __hip_atomic_fetch_add((LAS unsigned*)(smem + LDS_EV + e * 4), 1u, __ATOMIC_RELAXED, __HIP_MEMORY_SCOPE_WORKGROUP);
}
__device__ __forceinline__ unsigned ev_peek(char* smem, int e) {
  unsigned v = __hip_atomic_load((LAS unsigned*)(smem + LDS_EV + e * 4), __ATOMIC_RELAXED, __HIP_MEMORY_SCOPE_WORKGROUP);
  return __builtin_amdgcn_readfirstlane(v);
}
__device__ __forceinline__ void ev_wait(char* smem, int e, unsigned target) {
  while (ev_peek(smem, e) < target) __builtin_amdgcn_s_sleep(1);
  asm volatile("" ::: "memory");
  SB();
}
#define BAR() do { __builtin_amdgcn_s_barrier(); asm volatile("" ::: "memory"); SB(); } while (0)
__device__ __forceinline__ u32x2 add_res(f32x4 y, u32x2 r) {
  const unsigned y0 = pk_bf16(y[0], y[1]), y1 = pk_bf16(y[2], y[3]);
  return u32x2{pk_bf16(bf_lo(r[0]) + bf_lo(y0), bf_hi(r[0]) + bf_hi(y0)), pk_bf16(bf_lo(r[1]) + bf_lo(y1), bf_hi(r[1]) + bf_hi(y1))};
}

__global__ __launch_bounds__(512, 2) void k_main(char* __restrict__ ws_, const char* __restrict__ xin, char* __restrict__ out) {
  extern __shared__ __attribute__((aligned(16))) char smem[];
  Ctx cx;
  cx.ws = ws_;
  cx.rs = __builtin_amdgcn_make_buffer_rsrc(ws_, 0, 0x7fffffff, 0x00020000);
  const __amdgpu_buffer_rsrc_t rx = __builtin_amdgcn_make_buffer_rsrc((void*)xin, 0, S_ * D_ * 2, 0x00020000);
  const __amdgpu_buffer_rsrc_t ro = __builtin_amdgcn_make_buffer_rsrc(out, 0, S_ * D_ * 2, 0x00020000);
  if (threadIdx.x < 64) *(unsigned*)(smem + LDS_EV + threadIdx.x * 4) = 0u;

#pragma unroll 1
  for (int L = 0; L < 2; ++L) {
    int tid = threadIdx.x, b = blockIdx.x;
    asm volatile("" : "+v"(tid));
    asm volatile("" : "+s"(b));
    const int lane = tid & 63, wave = __builtin_amdgcn_readfirstlane(tid >> 6);
    const int xcd = b & 7, rr = b >> 3;
    const int p = MAPMODE == 0 ? xcd * 2 + (rr >> 4) : MAPMODE == 1 ? (xcd & 3) * 4 + (rr >> 3) : (xcd & 1) * 8 + (rr >> 2);
    const int j = MAPMODE == 0 ? (rr & 15) : MAPMODE == 1 ? (xcd >> 2) * 8 + (rr & 7) : (xcd >> 1) * 4 + (rr & 3);
    const int tok = lane & 15, g = lane >> 4;
    const bool projwg = (j & 7) < 7;
    const int qq = j >> 3, nblk = projwg ? (j & 7) : 6;
    const unsigned l16 = lane * 16;
    const unsigned r8t = (unsigned)(((g >> 1) * 16 + tok) * 16 + (g & 1) * 8);
    const unsigned r8r = (unsigned)(tok * (D_ * 2) + g * 8);
    const int tfw = p * 8 + wave;
    const unsigned M1 = (unsigned)(L + 1);
    char* const RW = smem + (L ? 0 : LDS_REG);
    char* const RA = smem + (L ? LDS_REG : 0);
    const __amdgpu_buffer_rsrc_t rsx = L ? cx.rs : rx;
    const unsigned xbase = L ? OFF_X + XT_BYTES : 0u;
    STAMP(0);
    if (L == 0) {
      if (wave < 7) {
#pragma unroll
        for (int i = 0; i < 12; ++i) dma<0>(cx, l16, OFF_W1 + (unsigned)(j * 84 + wave * 12 + i) * 1024, smem + LDS_REG + (wave * 12 + i) * 1024);
      }
      SB();
    } else {
      if (wave == 7) poll16(cx, flag_line(L - 1, F_X2, p, j), PROJ14, lane);
      BAR();
    }
    STAMP(1);
    u32x4 qf;
    {
      u32x4 a[KS];
      const unsigned va = L ? l16 : (unsigned)(tok * (D_ * 2) + g * 16), sst = L ? 1024u : 64u;
#pragma unroll
      for (int s = 0; s < KS; ++s) a[s] = __builtin_amdgcn_raw_buffer_load_b128(rsx, va, xbase + (unsigned)tfw * 14336u + s * sst, 16);
      SB();
      if (L == 0) {
        if (wave == 7) {
          const unsigned o1 = OFF_CS1 + (unsigned)(j * 96) * 4, o4 = OFF_CSGU + (unsigned)(j * 7 * 32) * 4;
          const unsigned lq = lane < 24 ? l16 : 0u, lg = lane < 56 ? l16 : 0u;
          u32x4 q0 = ld_w(cx, lq, o1), q1 = ld_w(cx, lq, o1 + 8192), g0 = ld_w(cx, lg, o4), g1 = ld_w(cx, lg, o4 + 16384);
          if (lane < 24) *(f32x4*)(smem + LDS_TAB + 2048 + l16) = __builtin_bit_cast(f32x4, q0) + __builtin_bit_cast(f32x4, q1);
          if (lane < 56) *(f32x4*)(smem + LDS_TAB + 1024 + l16) = __builtin_bit_cast(f32x4, g0) + __builtin_bit_cast(f32x4, g1);
        } else {
          WAIT_VM(14);
        }
        BAR_LDS();
        SB();
      }
      STAMP(2);
      auto w1tile = [&](int ti) { return *(const u32x4*)((ti < 56 ? RW + ti * 1024 : smem + LDS_W1B + (ti - 56) * 1024) + l16); };
      f32x2 st;
      {
        f32x4 acc[4], gram = {0, 0, 0, 0};
#pragma unroll
        for (int c = 0; c < 4; ++c) acc[c] = f32x4{0, 0, 0, 0};
        u32x4 wb[3][4];
#pragma unroll
        for (int s = 0; s < 2; ++s)
#pragma unroll
          for (int c = 0; c < 4; ++c) wb[s][c] = w1tile(s * 6 + 2 + c);
#pragma unroll
        for (int s = 0; s < KS; ++s) {
          if (s + 2 < KS) {
#pragma unroll
            for (int c = 0; c < 4; ++c) wb[(s + 2) % 3][c] = w1tile((s + 2) * 6 + 2 + c);
          }
          gram = mfma16(a[s], a[s], gram);
#pragma unroll
          for (int c = 0; c < 4; ++c) acc[c] = mfma16(wb[s % 3][c], a[s], acc[c]);
          SB();
        }
        STAMP(3);
        st = stat_finish(red_add_g(g == 3 ? acc[1][0] : 0.f), gram, tok, g);
        const float mi = st[0] * st[1];
        unsigned pk[4][2];
#pragma unroll
        for (int c = 0; c < 4; ++c) {
          const f32x4 csum = *(const f32x4*)(smem + LDS_TAB + 2048 + (c + 2) * 64 + g * 16);
          float v[4];
#pragma unroll
          for (int r = 0; r < 4; ++r) v[r] = fmaf(acc[c][r], st[1], -mi * csum[r]);
          if (c == 1 && g == 3) v[0] = 0.0f;
          if (c == 3 && g == 3) v[0] = 1.0f;
          pk[c][0] = pk_bf16(v[0], v[1]);
          pk[c][1] = pk_bf16(v[2], v[3]);
        }
        u32x4 kf = {pk[0][0], pk[0][1], pk[1][0], pk[1][1]};
        u32x4 vf = (g & 1) ? u32x4{pk[3][0], pk[3][1], pk[2][0], pk[2][1]} : u32x4{pk[2][0], pk[2][1], pk[3][0], pk[3][1]};
        const unsigned vslot = (unsigned)(g * 256 + ((tok * 16 + (g >> 1) * 128) & 255));
        st_sc1(cx, l16, OFF_K + L * KV_BYTES + (unsigned)(j * 128 + tfw) * 1024, kf);
        st_sc1(cx, vslot, OFF_V + L * KV_BYTES + (unsigned)(j * 128 + tfw) * 1024, vf);
        *(u32x4*)(RA + (16 + wave) * 1024 + l16) = kf;
        *(u32x4*)(RA + (24 + 16 + wave) * 1024 + vslot) = vf;
        if (p < 2) {
          u32x4 z = {0, 0, 0, 0};
          asm volatile("" : "+v"(z));
#pragma unroll
          for (int i = 0; i < 2; ++i) {
            const int ti = wave * 2 + i;
            if (p * 8 - 16 + ti < 0) {
              *(u32x4*)(RA + ti * 1024 + l16) = z;
              *(u32x4*)(RA + (24 + ti) * 1024 + l16) = z;
            }
          }
        }
        WAIT_VM(0);
        BAR_LDS();
      }
      STAMP(4);
      if (wave == 7 && lane < 2 && p + 1 + lane < 16)
        __hip_atomic_store((gu32*)(cx.ws + flag_line(L, F_KV, p + 1 + lane, j) + lane * 4), 1u, __ATOMIC_RELAXED, __HIP_MEMORY_SCOPE_AGENT);
      SB();
      {
        f32x4 acc[2] = {f32x4{0, 0, 0, 0}, f32x4{0, 0, 0, 0}};
        u32x4 wb[4][2];
#pragma unroll
        for (int s = 0; s < 3; ++s) { wb[s][0] = w1tile(s * 6); wb[s][1] = w1tile(s * 6 + 1); }
#pragma unroll
        for (int s = 0; s < KS; ++s) {
          if (s + 3 < KS) { wb[(s + 3) & 3][0] = w1tile((s + 3) * 6); wb[(s + 3) & 3][1] = w1tile((s + 3) * 6 + 1); }
          acc[0] = mfma16(wb[s & 3][0], a[s], acc[0]);
          acc[1] = mfma16(wb[s & 3][1], a[s], acc[1]);
          SB();
        }
        const float mi = st[0] * st[1];
#pragma unroll
        for (int c = 0; c < 2; ++c) {
          const f32x4 csum = *(const f32x4*)(smem + LDS_TAB + 2048 + c * 64 + g * 16);
          float v[4];
#pragma unroll
          for (int r = 0; r < 4; ++r) v[r] = fmaf(acc[c][r], st[1], -mi * csum[r]);
          qf[2 * c] = pk_bf16(v[0], v[1]);
          qf[2 * c + 1] = pk_bf16(v[2], v[3]);
        }
      }
    }
    if (wave == 7 && p >= 1) poll16(cx, flag_line(L, F_KV, p, j), p >= 2 ? 3u : 1u, lane);
    const int Fi = j * 7 + (wave < 7 ? wave : 0);
    constexpr int W4A = 6, W4E = 10;
    u32x4 w4[KS][2];
    BAR();
#pragma unroll
    for (int i = 0; i < 2; ++i) {
      const int ti = wave * 2 + i, T = p * 8 - 16 + ti;
      if (T >= 0) {
        dma<16>(cx, l16, OFF_K + L * KV_BYTES + (unsigned)(j * 128 + T) * 1024, RA + ti * 1024);
        dma<16>(cx, l16, OFF_V + L * KV_BYTES + (unsigned)(j * 128 + T) * 1024, RA + (24 + ti) * 1024);
      }
    }
    WAIT_VM(0);
    BAR();
    STAMP(5);
    if (projwg) {
#pragma unroll
      for (int i = 0; i < 7; ++i) dma<0>(cx, l16, OFF_WO + (unsigned)(nblk * 56 + wave * 7 + i) * 1024, RW + (wave * 7 + i) * 1024);
    }
    SB();
    attn_body(cx, RA, qf, wave, lane, p, OFF_O + L * XT_BYTES + (unsigned)(tfw * KS) * 1024, j, [&](int s) {
      w4[s][0] = ld_w(cx, l16, OFF_WGU + (unsigned)((Fi * KS + s) * 2) * 1024);
      w4[s][1] = ld_w(cx, l16, OFF_WGU + (unsigned)((Fi * KS + s) * 2 + 1) * 1024);
    });
    STAMP(6);
    WAIT_VM(0);
    BAR();
    STAMP(7);
    if (wave == 7) {
      signal16(cx, L, F_O, p, j, lane);
      if (projwg) poll16(cx, flag_line(L, F_O, p, j), ALL16, lane);
    }
    if (wave < 7) {
#pragma unroll
      for (int s = W4A; s < W4E; ++s) {
        w4[s][0] = ld_w(cx, l16, OFF_WGU + (unsigned)((Fi * KS + s) * 2) * 1024);
        w4[s][1] = ld_w(cx, l16, OFF_WGU + (unsigned)((Fi * KS + s) * 2 + 1) * 1024);
      }
    } else {
#pragma unroll
      for (int s = W4A; s < W4E; ++s) { w4[s][0] = u32x4{0, 0, 0, 0}; w4[s][1] = u32x4{0, 0, 0, 0}; }
    }
    SB();
    if (projwg) {
      const int t3 = wave & 3, kh = wave >> 2;
      const int tf3 = p * 8 + qq * 4 + t3;
      const char* wlp = RW + kh * 28 * 1024 + l16;
      u32x4 wf[3][4];
#pragma unroll
      for (int i = 0; i < 2; ++i)
#pragma unroll
        for (int jj = 0; jj < 4; ++jj) wf[i][jj] = *(const u32x4*)(wlp + (i * 4 + jj) * 1024);
      u32x2 rs[2];
#pragma unroll
      for (int q = 0; q < 2; ++q) {
        const int jj = kh * 2 + q;
        const unsigned so = L ? (unsigned)(tf3 * KS + 2 * nblk + kh) * 1024 + q * 512 : (unsigned)tf3 * 14336u + nblk * 128 + jj * 32;
        rs[q] = __builtin_amdgcn_raw_buffer_load_b64(rsx, L ? r8t : r8r, xbase + so, 16);
      }
      BAR();
      STAMP(8);
      u32x4 a3[7];
#pragma unroll
      for (int i = 0; i < 7; ++i) a3[i] = ld_sc1(cx, l16, OFF_O + L * XT_BYTES + (unsigned)(tf3 * KS + kh * 7 + i) * 1024);
      SB();
      f32x4 acc[4];
#pragma unroll
      for (int jj = 0; jj < 4; ++jj) acc[jj] = f32x4{0, 0, 0, 0};
#pragma unroll
      for (int i = 0; i < 7; ++i) {
        if (i + 2 < 7) {
#pragma unroll
          for (int jj = 0; jj < 4; ++jj) wf[(i + 2) % 3][jj] = *(const u32x4*)(wlp + ((i + 2) * 4 + jj) * 1024);
        }
#pragma unroll
        for (int jj = 0; jj < 4; ++jj) acc[jj] = mfma16(wf[i % 3][jj], a3[i], acc[jj]);
        SB();
      }
      char* pp3 = smem + LDS_P3 + l16;
      *(f32x4*)(pp3 + (wave * 2 + 0) * 1024) = kh ? acc[0] : acc[2];
      *(f32x4*)(pp3 + (wave * 2 + 1) * 1024) = kh ? acc[1] : acc[3];
      BAR_LDS();
#pragma unroll
      for (int q = 0; q < 2; ++q) {
        f32x4 y = (kh ? acc[2 + q] : acc[q]) + *(const f32x4*)(pp3 + ((wave ^ 4) * 2 + q) * 1024);
        st_sc1_b64(cx, r8t, OFF_X1 + L * XT_BYTES + (unsigned)(tf3 * KS + 2 * nblk + kh) * 1024 + q * 512, add_res(y, rs[q]));
      }
      WAIT_VM(0);
    }
    BAR();
    STAMP(9);
    u32x4 w5[8][4];
    const unsigned w5base = OFF_WD + (unsigned)((nblk * 56 + wave * 8) * 4) * 1024;
    if (wave == 7) {
      if (projwg) signal16(cx, L, F_X1, p, j, lane);
      poll16(cx, flag_line(L, F_X1, p, j), PROJ14, lane);
    }
    if (wave < 7) {
#pragma unroll
      for (int s = W4E; s < KS; ++s) {
        w4[s][0] = ld_w(cx, l16, OFF_WGU + (unsigned)((Fi * KS + s) * 2) * 1024);
        w4[s][1] = ld_w(cx, l16, OFF_WGU + (unsigned)((Fi * KS + s) * 2 + 1) * 1024);
      }
    } else {
#pragma unroll
      for (int s = W4E; s < KS; ++s) { w4[s][0] = u32x4{0, 0, 0, 0}; w4[s][1] = u32x4{0, 0, 0, 0}; }
    }
    BAR();
    STAMP(10);
#pragma unroll
    for (int i = 0; i < 7; ++i) dma<16>(cx, l16, OFF_X1 + L * XT_BYTES + (unsigned)(p * 8 * KS + wave * 7 + i) * 1024, RA + (wave * 7 + i) * 1024);
    SB();
    asm volatile("" ::: "memory");
#pragma unroll
    for (int i = 0; i < 7; ++i) dma<16>(cx, l16, OFF_X1 + L * XT_BYTES + (unsigned)((p * 8 + 4) * KS + wave * 7 + i) * 1024, RW + (wave * 7 + i) * 1024);
    WAIT_VM(7);
    BAR();
    STAMP(11);
    const int tf5 = p * 8 + qq * 2;
    const int ft = wave >> 2, fj = wave & 3;
    const unsigned fso = (unsigned)(2 * nblk + (fj >> 1)) * 1024 + (fj & 1) * 512;
    u32x4 hb[8][2];
    bool hb_issued = false;
    if (wave < 7) {
#pragma unroll
      for (int hh = 0; hh < 2; ++hh) {
        const char* xt = (hh == 0 ? RA : RW) + l16;
        f32x4 acc[4][2];
#pragma unroll
        for (int t = 0; t < 4; ++t) { acc[t][0] = f32x4{0, 0, 0, 0}; acc[t][1] = f32x4{0, 0, 0, 0}; }
        u32x4 xb[3][4];
        auto rd = [&](int s) {
#pragma unroll
          for (int t = 0; t < 4; ++t) xb[s % 3][t] = *(const u32x4*)(xt + (t * KS + s) * 1024);
        };
        rd(0); rd(1);
        SB();
#pragma unroll
        for (int s = 0; s < KS; ++s) {
          if (s + 2 < KS) rd(s + 2);
#pragma unroll
          for (int t = 0; t < 4; ++t) {
            acc[t][0] = mfma16(w4[s][0], xb[s % 3][t], acc[t][0]);
            acc[t][1] = mfma16(w4[s][1], xb[s % 3][t], acc[t][1]);
          }
          SB();
          if (hh == 0 && s == 3) { WAIT_VM(0); ev_post(smem, E_X1B, lane); SB(); }
          if (hh == 0 && s >= 6) { w5[(s - 6) >> 2][(s - 6) & 3] = ld_w(cx, l16, w5base + (s - 6) * 1024); SB(); }
          if (hh == 1 && s < 12) {
#pragma unroll
            for (int q = 0; q < 2; ++q) { const int idx = 8 + s * 2 + q; w5[idx >> 2][idx & 3] = ld_w(cx, l16, w5base + idx * 1024); }
            SB();
          }
          if (hh == 1 && s == 2) {
            WAIT_VM(6);
            if (ev_arrive(smem, E_HD0, lane) == 7 * M1 - 1) signal16(cx, L, F_HA, p, j, lane);
            SB();
          }
        }
        STAMP(12 + hh * 2);
        ev_post(smem, E_T4A + hh, lane);
        ev_wait(smem, E_ST0 + hh, M1);
        const f32x4 cg = *(const f32x4*)(smem + LDS_TAB + 1024 + wave * 128 + g * 16);
        const f32x4 cu = *(const f32x4*)(smem + LDS_TAB + 1024 + wave * 128 + 64 + g * 16);
        unsigned hv[4][2];
#pragma unroll
        for (int t = 0; t < 4; ++t) {
          const f32x2 s2 = *(const f32x2*)(smem + LDS_TAB + ((hh * 4 + t) * 16 + tok) * 8);
          float hq[4];
          float nm = -s2[0], i2 = s2[1];
#pragma unroll
          for (int r = 0; r < 4; ++r) {
            float tg = fmaf(nm, cg[r], acc[t][0][r]), tu = fmaf(nm, cu[r], acc[t][1][r]);
            asm volatile("" : "+v"(tg), "+v"(tu));
            tg *= i2;
            asm volatile("" : "+v"(tg));
            hq[r] = tg * tu;
          }
          hv[t][0] = pk_bf16(hq[0], hq[1]);
          hv[t][1] = pk_bf16(hq[2], hq[3]);
        }
        const unsigned hlane = (unsigned)((g & 1) * (KSF * 1024) + ((Fi & 1) * 2 + (g >> 1)) * 256 + tok * 16);
#pragma unroll
        for (int u = 0; u < 2; ++u) {
          auto s0 = __builtin_amdgcn_permlane16_swap(hv[2 * u][0], hv[2 * u + 1][0], false, false);
          auto s1 = __builtin_amdgcn_permlane16_swap(hv[2 * u][1], hv[2 * u + 1][1], false, false);
          u32x4 o = {s0[0], s1[0], s0[1], s1[1]};
          st_sc1(cx, hlane, OFF_H + L * H_BYTES + (unsigned)((p * 8 + hh * 4 + 2 * u) * KSF + (Fi >> 1)) * 1024, o);
        }
        SB();
        if (hh == 0) { ev_wait(smem, E_X1B, 8 * M1); STAMP(13); }
      }
      hb_issued = projwg && ev_peek(smem, E_HR0) >= M1;
      asm volatile("" ::: "memory");
      if (hb_issued) {
        const unsigned hbase = OFF_H + L * H_BYTES + (unsigned)(tf5 * KSF + wave * 8) * 1024;
#pragma unroll
        for (int i = 0; i < 8; ++i)
#pragma unroll
          for (int t = 0; t < 2; ++t) hb[i][t] = ld_sc1(cx, l16, hbase + (unsigned)(t * KSF + i) * 1024);
        SB();
        WAIT_VM(16);
      } else {
#pragma unroll
        for (int i = 0; i < 8; ++i) { hb[i][0] = u32x4{0, 0, 0, 0}; hb[i][1] = u32x4{0, 0, 0, 0}; }
        WAIT_VM(0);
      }
      if (ev_arrive(smem, E_HD1, lane) == 7 * M1 - 1) signal16(cx, L, F_HB, p, j, lane);
      STAMP(15);
    } else {
#pragma unroll
      for (int i = 0; i < 32; ++i) w5[i >> 2][i & 3] = u32x4{0, 0, 0, 0};
#pragma unroll
      for (int i = 0; i < 8; ++i) { hb[i][0] = u32x4{0, 0, 0, 0}; hb[i][1] = u32x4{0, 0, 0, 0}; }
      __builtin_amdgcn_s_setprio(3);
      const u32x4 ones = {0x3f803f80u, 0x3f803f80u, 0x3f803f80u, 0x3f803f80u};
#pragma unroll
      for (int hh = 0; hh < 2; ++hh) {
        if (hh == 1) { WAIT_VM(0); ev_post(smem, E_X1B, lane); ev_wait(smem, E_X1B, 8 * M1); }
        const char* xt = (hh == 0 ? RA : RW) + l16;
        f32x4 sm[4], gr[4];
#pragma unroll
        for (int t = 0; t < 4; ++t) { sm[t] = f32x4{0, 0, 0, 0}; gr[t] = f32x4{0, 0, 0, 0}; }
        u32x4 xb[3][4];
        auto rd = [&](int s) {
#pragma unroll
          for (int t = 0; t < 4; ++t) xb[s % 3][t] = *(const u32x4*)(xt + (t * KS + s) * 1024);
        };
        rd(0); rd(1);
        SB();
#pragma unroll
        for (int s = 0; s < KS; ++s) {
          if (s + 2 < KS) rd(s + 2);
#pragma unroll
          for (int t = 0; t < 4; ++t) {
            sm[t] = mfma16(ones, xb[s % 3][t], sm[t]);
            gr[t] = mfma16(xb[s % 3][t], xb[s % 3][t], gr[t]);
          }
          SB();
        }
#pragma unroll
        for (int t = 0; t < 4; ++t) {
          const f32x2 st = stat_finish(sm[t][0], gr[t], tok, g);
          if (g == 0) *(f32x2*)(smem + LDS_TAB + ((hh * 4 + t) * 16 + tok) * 8) = f32x2{st[0], st[1] * st[1]};
        }
        ev_post(smem, E_ST0 + hh, lane);
        STAMP(12 + hh);
      }
      __builtin_amdgcn_s_setprio(0);
      if (L == 0) {
        ev_wait(smem, E_T4A, 7 * M1);
#pragma unroll 1
        for (int i = 0; i < 84; i += 4) {
#pragma unroll
          for (int q = 0; q < 4; ++q)
            dma<0>(cx, l16, OFF_W1 + (unsigned)(j * 84 + i + q) * 1024, i < 56 ? RA + (i + q) * 1024 : smem + LDS_W1B + (i - 56 + q) * 1024);
        }
        SB();
      }
      STAMP(14);
      if (projwg) {
        poll16(cx, flag_line(L, F_HA, p, j), ALL16, lane);
        ev_post(smem, E_HR0, lane);
        STAMP(15);
        poll16(cx, flag_line(L, F_HB, p, j), ALL16, lane);
        ev_post(smem, E_HR1, lane);
        STAMP(16);
      }
    }
    if (projwg) {
      u32x2 r5[2];
#pragma unroll
      for (int hh = 0; hh < 2; ++hh)
        r5[hh] = __builtin_amdgcn_raw_buffer_load_b64(cx.rs, r8t, OFF_X1 + L * XT_BYTES + (unsigned)((tf5 + hh * 4 + ft) * KS) * 1024 + fso, 16);
#pragma unroll
      for (int hh = 0; hh < 2; ++hh) {
        if (wave < 7) {
          if (!hb_issued) {
            ev_wait(smem, E_HR0 + hh, M1);
            const unsigned hbase = OFF_H + L * H_BYTES + (unsigned)((tf5 + hh * 4) * KSF + wave * 8) * 1024;
#pragma unroll
            for (int i = 0; i < 8; ++i)
#pragma unroll
              for (int t = 0; t < 2; ++t) hb[i][t] = ld_sc1(cx, l16, hbase + (unsigned)(t * KSF + i) * 1024);
          }
          STAMP(16 + hh * 3);
          SB();
          f32x4 acc[4][2];
#pragma unroll
          for (int jj = 0; jj < 4; ++jj) { acc[jj][0] = f32x4{0, 0, 0, 0}; acc[jj][1] = f32x4{0, 0, 0, 0}; }
#pragma unroll
          for (int i = 0; i < 8; ++i)
#pragma unroll
            for (int jj = 0; jj < 4; ++jj)
#pragma unroll
              for (int t = 0; t < 2; ++t) acc[jj][t] = mfma16(w5[i][jj], hb[i][t], acc[jj][t]);
          STAMP(17 + hh * 3);
          if (hh == 0) { ev_wait(smem, E_T4D, 7 * M1); ev_wait(smem, E_ST1, M1); } else ev_wait(smem, E_P5F, 8 * M1);
#pragma unroll
          for (int t = 0; t < 2; ++t)
#pragma unroll
            for (int jj = 0; jj < 4; ++jj) *(f32x4*)(RW + (wave * 8 + t * 4 + jj) * 1024 + l16) = acc[jj][t];
          ev_post(smem, E_P5P0 + hh, lane);
          if (hh == 0) {
            ev_wait(smem, E_HR1, M1);
            hb_issued = true;
            {
              const unsigned hbase = OFF_H + L * H_BYTES + (unsigned)((tf5 + 4) * KSF + wave * 8) * 1024;
#pragma unroll
              for (int i = 0; i < 8; ++i)
#pragma unroll
                for (int t = 0; t < 2; ++t) hb[i][t] = ld_sc1(cx, l16, hbase + (unsigned)(t * KSF + i) * 1024);
            }
            SB();
          }
        }
        ev_wait(smem, E_P5P0 + hh, 7 * M1);
        STAMP(18 + hh * 3);
        f32x4 y = *(const f32x4*)(RW + wave * 1024 + l16);
#pragma unroll
        for (int sw = 1; sw < 7; ++sw) y += *(const f32x4*)(RW + (sw * 8 + wave) * 1024 + l16);
        if (hh == 0) ev_post(smem, E_P5F, lane);
        const u32x2 o = add_res(y, r5[hh]);
        const int tf = tf5 + hh * 4 + ft;
        if (L == 0) st_sc1_b64(cx, r8t, OFF_X + XT_BYTES + (unsigned)(tf * KS) * 1024 + fso, o);
        else __builtin_amdgcn_raw_buffer_store_b64(o, ro, r8r, (unsigned)tf * 14336u + nblk * 128 + fj * 32, 0);
      }
      if (L == 0) {
        WAIT_VM(0);
        if (ev_arrive(smem, E_X2D, lane) == 7) signal16(cx, L, F_X2, p, j, lane);
      }
      STAMP(22);
    }
    WAIT_VM(0);
  }
}

extern "C" void kernel_launch(void* const* d_in, const int* in_sizes, int n_in, void* d_out, int out_size, void* d_ws,
                              size_t ws_size, hipStream_t stream) {
  const uint16_t* x = (const uint16_t*)d_in[0];
  const uint16_t* wqkv = (const uint16_t*)d_in[1];
  const uint16_t* wo = (const uint16_t*)d_in[2];
  const uint16_t* wg = (const uint16_t*)d_in[4];
  const uint16_t* wu = (const uint16_t*)d_in[5];
  const uint16_t* wd = (const uint16_t*)d_in[6];
  char* ws = (char*)d_ws;
  constexpr int LP = 224 * 192;
  (void)hipFuncSetAttribute((const void*)k_main, hipFuncAttributeMaxDynamicSharedMemorySize, LDS_TOTAL);
  k_prep<<<222, 512, LP, stream>>>(x, wqkv, wo, wg, wu, wd, ws);
  k_main<<<256, 512, LDS_TOTAL, stream>>>(ws, (const char*)x, (char*)d_out);
}
```

```cpp
#include <hip/hip_runtime.h>
#include <hip/hip_bf16.h>
#include <stdint.h>

typedef __attribute__((ext_vector_type(8))) short s16x8;
typedef __attribute__((ext_vector_type(4))) short s16x4;
typedef __attribute__((ext_vector_type(4))) float f32x4;
typedef __attribute__((ext_vector_type(2))) float f32x2;
typedef __attribute__((ext_vector_type(2))) __bf16 bf16x2;
typedef __attribute__((ext_vector_type(4))) unsigned u32x4;
typedef __attribute__((ext_vector_type(2))) unsigned u32x2;
#define LAS __attribute__((address_space(3)))
typedef __attribute__((address_space(1))) unsigned gu32;

static constexpr int S_ = 2048, D_ = 448, F_ = 1792, NH = 16, DH = 28;
static constexpr int KS = 14, KSF = 56;

static constexpr unsigned OFF_CNT = 0;
static constexpr unsigned OFF_TMO = 262144;
static constexpr unsigned FLAG_BYTES = 262144 + 1024;
static constexpr unsigned OFF_W1 = 327680;
static constexpr unsigned OFF_CS1 = OFF_W1 + 16 * 14 * 6 * 1024;
static constexpr unsigned OFF_WO = OFF_CS1 + 16384;
static constexpr unsigned OFF_WGU = OFF_WO + 7 * 14 * 4 * 1024;
static constexpr unsigned OFF_CSGU = OFF_WGU + 112 * 14 * 2 * 1024;
static constexpr unsigned OFF_WD = OFF_CSGU + 32768;
static constexpr unsigned XT_BYTES = 128 * 14 * 1024;
static constexpr unsigned OFF_X = OFF_WD + 7 * 56 * 4 * 1024;
static constexpr unsigned OFF_X1 = OFF_X + 2 * XT_BYTES;
static constexpr unsigned OFF_O = OFF_X1 + 2 * XT_BYTES;
static constexpr unsigned KV_BYTES = 16 * 128 * 1024;
static constexpr unsigned OFF_K = OFF_O + 2 * XT_BYTES;
static constexpr unsigned OFF_V = OFF_K + 2 * KV_BYTES;
static constexpr unsigned H_BYTES = 128 * 56 * 1024;
static constexpr unsigned OFF_H = OFF_V + 2 * KV_BYTES;
static constexpr unsigned ST_BYTES = 2048 * 64;
static constexpr unsigned OFF_STX = OFF_H + 2 * H_BYTES;
static constexpr unsigned OFF_STX1 = OFF_STX + 2 * ST_BYTES;

__device__ __forceinline__ float bf_lo(unsigned u) { return __uint_as_float(u << 16); }
__device__ __forceinline__ float bf_hi(unsigned u) { return __uint_as_float(u & 0xffff0000u); }
__device__ __forceinline__ unsigned pk_bf16(float a, float b) {
  f32x2 f = {a, b};
  bf16x2 r = __builtin_convertvector(f, bf16x2);
  return __builtin_bit_cast(unsigned, r);
}
__device__ __forceinline__ float rnd_bf16(float a) { return bf_lo(pk_bf16(a, 0.f)); }
__device__ __forceinline__ float red_add_g(float v) {
  auto p = __builtin_amdgcn_permlane16_swap(__float_as_uint(v), __float_as_uint(v), false, false);
  v = __uint_as_float(p[0]) + __uint_as_float(p[1]);
  auto q = __builtin_amdgcn_permlane32_swap(__float_as_uint(v), __float_as_uint(v), false, false);
  return __uint_as_float(q[0]) + __uint_as_float(q[1]);
}
__device__ __forceinline__ float red_max_g(float v) {
  auto p = __builtin_amdgcn_permlane16_swap(__float_as_uint(v), __float_as_uint(v), false, false);
  v = fmaxf(__uint_as_float(p[0]), __uint_as_float(p[1]));
  auto q = __builtin_amdgcn_permlane32_swap(__float_as_uint(v), __float_as_uint(v), false, false);
  return fmaxf(__uint_as_float(q[0]), __uint_as_float(q[1]));
}
__device__ __forceinline__ f32x4 mfma16(u32x4 a, u32x4 b, f32x4 c) {
  return __builtin_amdgcn_mfma_f32_16x16x32_bf16(__builtin_bit_cast(s16x8, a), __builtin_bit_cast(s16x8, b), c, 0, 0, 0);
}
__device__ __forceinline__ u32x4 tr_frag(const char* p0, const char* p1) {
  s16x4 lo = __builtin_amdgcn_ds_read_tr16_b64_v4i16((LAS s16x4*)p0);
  s16x4 hi = __builtin_amdgcn_ds_read_tr16_b64_v4i16((LAS s16x4*)p1);
  u32x2 l2 = __builtin_bit_cast(u32x2, lo), h2 = __builtin_bit_cast(u32x2, hi);
  return u32x4{l2[0], l2[1], h2[0], h2[1]};
}
#define SB() __builtin_amdgcn_sched_barrier(0)
#define STAMP(i)
#define WAIT_VM(n) asm volatile("s_waitcnt vmcnt(" #n ")" ::: "memory")
#define BAR_LDS() do { asm volatile("s_waitcnt lgkmcnt(0)" ::: "memory"); __builtin_amdgcn_s_barrier(); asm volatile("" ::: "memory"); } while (0)

__device__ __forceinline__ f32x2 stat_finish(float sum, f32x4 gram, int tok, int g) {
  float d = (tok & 2) ? ((tok & 1) ? gram[3] : gram[2]) : ((tok & 1) ? gram[1] : gram[0]);
  d = (g == (tok >> 2)) ? d : 0.f;
  const float sq = red_add_g(d);
  const float mean = sum * (1.0f / D_);
  const float var = fmaxf(fmaf(-mean, mean, sq * (1.0f / D_)), 0.f);
  const float mb = rnd_bf16(mean);
  const float den = rnd_bf16(__builtin_amdgcn_sqrtf(rnd_bf16(rnd_bf16(var) + 1e-5f)));
  return f32x2{mb, __builtin_amdgcn_rcpf(den)};
}

__device__ __forceinline__ void tile_block(const uint16_t* __restrict__ src, int ld, int col0, const int (&rows)[8], char* dst,
                                           int jjstride, int lane, u32x4 (&o)[4], bool consec = false) {
  const int i = lane & 15;
  u32x2 v[8];
#pragma unroll
  for (int e = 0; e < 8; ++e) v[e] = *(const u32x2*)(src + (size_t)rows[e] * ld + col0 + 4 * i);
#pragma unroll
  for (int jj = 0; jj < 4; ++jj) {
#pragma unroll
    for (int d = 0; d < 4; ++d)
      o[jj][d] = __builtin_amdgcn_perm(v[2 * d + 1][jj >> 1], v[2 * d][jj >> 1], (jj & 1) ? 0x07060302u : 0x05040100u);
    if (consec) *(u32x4*)(dst + (size_t)(i >> 2) * jjstride + ((lane >> 4) * 16 + 4 * (i & 3) + jj) * 16) = o[jj];
    else *(u32x4*)(dst + (size_t)jj * jjstride + lane * 16) = o[jj];
  }
}

__global__ __launch_bounds__(512, 2) void k_prep(const uint16_t* __restrict__ x, const uint16_t* __restrict__ wqkv,
                                                 const uint16_t* __restrict__ wo, const uint16_t* __restrict__ wg,
                                                 const uint16_t* __restrict__ wu, const uint16_t* __restrict__ wd,
                                                 char* __restrict__ ws) {
  extern __shared__ __attribute__((aligned(16))) char smem[];
  const int tid = threadIdx.x, lane = tid & 63, wave = __builtin_amdgcn_readfirstlane(tid >> 6);
  const int b = blockIdx.x;
  const int g = lane >> 4;
  const u32x4 ones = {0x3f803f80u, 0x3f803f80u, 0x3f803f80u, 0x3f803f80u};
  if (b < 112) {
    const int kh = b & 1, which = (b >> 1) & 1, blk = b >> 2;
    if (wave < 7) {
      const int s = kh * 7 + wave;
      int rows[8];
#pragma unroll
      for (int e = 0; e < 8; ++e) rows[e] = 32 * s + 8 * g + e;
      u32x4 o[4];
      tile_block(which ? wu : wg, F_, blk * 64, rows, ws + OFF_WGU + (size_t)(((blk * 4) * 14 + s) * 2 + which) * 1024,
                 14 * 2 * 1024, lane, o);
#pragma unroll
      for (int jj = 0; jj < 4; ++jj) {
        f32x4 cs = mfma16(o[jj], ones, f32x4{0, 0, 0, 0});
        if ((lane & 15) == 0) *(f32x4*)(smem + ((wave * 4 + jj) * 4 + g) * 16) = cs;
      }
    }
    __syncthreads();
    if (tid < 64) {
      float s = 0.f;
#pragma unroll
      for (int w = 0; w < 7; ++w) s += *(const float*)(smem + (w * 64 + tid) * 4);
      const int jj = tid >> 4, row = tid & 15;
      *(float*)(ws + OFF_CSGU + (size_t)kh * 16384 + (size_t)(((blk * 4 + jj) * 2 + which) * 16 + row) * 4) = s;
    }
  } else if (b < 126) {
    const int t = b - 112, nblk = t >> 1;
    if (wave < 7) {
      const int s = (t & 1) * 7 + wave;
      int rows[8];
#pragma unroll
      for (int e = 0; e < 8; ++e) rows[e] = 32 * s + 8 * g + e;
      u32x4 o[4];
      tile_block(wo, D_, nblk * 64, rows, ws + OFF_WO + (size_t)((nblk * 14 + s) * 4) * 1024, 1024, lane, o, true);
    }
  } else if (b < 182) {
    const int t = b - 126, nblk = t >> 3;
    if (wave < 7) {
      const int s = (t & 7) * 7 + wave;
      const int Fi = 2 * s + (g >> 1);
      int rows[8];
#pragma unroll
      for (int e = 0; e < 8; ++e) rows[e] = 64 * (Fi >> 2) + 16 * (2 * (g & 1) + (e >> 2)) + 4 * (e & 3) + (Fi & 3);
      u32x4 o[4];
      tile_block(wd, D_, nblk * 64, rows, ws + OFF_WD + (size_t)((nblk * 56 + s) * 4) * 1024, 1024, lane, o, true);
    }
  } else if (b < 214) {
    const int head = (b - 182) >> 1, kh = (b - 182) & 1;
    u32x2 v[11];
#pragma unroll
    for (int it = 0; it < 11; ++it) {
      const int c = it * 512 + tid;
      const int row = c / 24, grp = c - row * 24;
      const int part = grp >> 3, gi = grp & 7;
      v[it] = u32x2{grp == 15 ? 0x00003f80u : 0u, 0u};
      if (gi < 7 && row < 224) v[it] = *(const u32x2*)(wqkv + (size_t)(kh * 224 + row) * (3 * D_) + part * D_ + head * DH + gi * 4);
    }
#pragma unroll
    for (int it = 0; it < 11; ++it) {
      const int c = it * 512 + tid;
      const int row = c / 24, grp = c - row * 24;
      int pp = (grp >> 2) + ((row >> 3) & 1);
      if (pp >= 6) pp -= 6;
      if (row < 224) *(u32x2*)(smem + row * 192 + pp * 32 + (grp & 3) * 8) = v[it];
    }
    __syncthreads();
    if (wave < 6) {
      const int c = wave;
      const int q4 = (lane & 15) >> 2, p4 = lane & 3;
      int pp = c + (g & 1);
      if (pp >= 6) pp -= 6;
      const char* base = smem + (8 * g + q4) * 192 + pp * 32 + p4 * 8;
      f32x4 cs = {0, 0, 0, 0};
#pragma unroll
      for (int s = 0; s < 7; ++s) {
        u32x4 wf = tr_frag(base + s * 32 * 192, base + s * 32 * 192 + 4 * 192);
        *(u32x4*)(ws + OFF_W1 + (size_t)((head * 14 + kh * 7 + s) * 6 + c) * 1024 + lane * 16) = wf;
        cs = mfma16(wf, ones, cs);
      }
      if ((lane & 15) == 0) *(f32x4*)(ws + OFF_CS1 + (size_t)kh * 8192 + (size_t)(head * 96 + c * 16 + 4 * g) * 4) = cs;
    }
  } else {
#pragma unroll 1
    for (unsigned o = ((b - 214) * 512 + tid) * 16; o < FLAG_BYTES; o += 8 * 512 * 16) *(u32x4*)(ws + o) = u32x4{0, 0, 0, 0};
  }
}

#define MAPMODE 2
static constexpr int LDS_REG = 57344;
static constexpr int LDS_W1B = 114688;
static constexpr int LDS_P3 = 143360;
static constexpr int LDS_TAB = 159744;
static constexpr int LDS_EV = 162816;
static constexpr int LDS_TOTAL = 163840;

struct Ctx {
  __amdgpu_buffer_rsrc_t rs;
  char* ws;
};
__device__ __forceinline__ u32x4 ld_sc1(const Ctx& c, unsigned voff, unsigned soff) { return __builtin_amdgcn_raw_buffer_load_b128(c.rs, voff, soff, 16); }
__device__ __forceinline__ u32x4 ld_w(const Ctx& c, unsigned voff, unsigned soff) { return __builtin_amdgcn_raw_buffer_load_b128(c.rs, voff, soff, 0); }
__device__ __forceinline__ void st_sc1(const Ctx& c, unsigned voff, unsigned soff, u32x4 v) { __builtin_amdgcn_raw_buffer_store_b128(v, c.rs, voff, soff, 16); }
__device__ __forceinline__ void st_sc1_b64(const Ctx& c, unsigned voff, unsigned soff, u32x2 v) { __builtin_amdgcn_raw_buffer_store_b64(v, c.rs, voff, soff, 16); }
template <int AUX>
__device__ __forceinline__ void dma(const Ctx& c, unsigned voff, unsigned soff, char* lds_uniform) {
  __builtin_amdgcn_raw_ptr_buffer_load_lds(c.rs, (LAS void*)lds_uniform, 16, voff, soff, 0, AUX);
}
__device__ __forceinline__ unsigned flag_line(int L, int e, int p, int c) { return OFF_CNT + (unsigned)((((L * 8 + e) * 16 + p) * 16 + c) * 64); }
__device__ __forceinline__ void signal16(const Ctx& c, int L, int e, int p, int j, int lane) {
  if (lane < 16) __hip_atomic_store((gu32*)(c.ws + flag_line(L, e, p, lane) + j * 4), 1u, __ATOMIC_RELAXED, __HIP_MEMORY_SCOPE_AGENT);
}
__device__ __forceinline__ void poll16(const Ctx& c, unsigned line, unsigned need, int lane) {
  gu32* f = (gu32*)(c.ws + line + (lane & 15) * 4);
  gu32* tmo = (gu32*)(c.ws + OFF_TMO);
  const bool skip = ((need >> (lane & 15)) & 1u) == 0u;
  unsigned spins = 0;
  for (;;) {
    const unsigned v = __hip_atomic_load(f, __ATOMIC_RELAXED, __HIP_MEMORY_SCOPE_AGENT);
    if (__all(skip || v != 0u)) break;
    ++spins;
    if ((spins & 1023u) == 0u) {
      if (spins > (1u << 20) || __hip_atomic_load(tmo, __ATOMIC_RELAXED, __HIP_MEMORY_SCOPE_AGENT) != 0u) {
        __hip_atomic_store(tmo, 1u, __ATOMIC_RELAXED, __HIP_MEMORY_SCOPE_AGENT);
        break;
      }
    }
  }
  __builtin_amdgcn_fence(__ATOMIC_ACQUIRE, "wavefront");
}
enum { F_KV, F_OA, F_OB, F_X1A, F_X1B, F_HA, F_HB, F_X2 };
static constexpr unsigned ALL16 = 0xffffu, PROJ14 = 0x7f7fu;

__device__ __forceinline__ float max3f(float a, float b, float c) { float r; asm("v_max3_f32 %0, %1, %2, %3" : "=v"(r) : "v"(a), "v"(b), "v"(c)); return r; }
template <typename PF>
__device__ __forceinline__ void attn_body(const Ctx& cx, const char* kv, u32x4 qf, int w, int lane, int p, unsigned ooff, int head, PF&& pf_) {
  const int tok = lane & 15, g = lane >> 4;
  const int t0 = w & ~1;
  const char* kl = kv + t0 * 1024 + lane * 16;
  f32x4 sc[18];
  {
    u32x4 kf[2][3];
#pragma unroll
    for (int q = 0; q < 3; ++q) kf[0][q] = *(const u32x4*)(kl + q * 1024);
#pragma unroll
    for (int gi = 0; gi < 6; ++gi) {
      if (gi + 1 < 6) {
#pragma unroll
        for (int q = 0; q < 3; ++q) kf[(gi + 1) & 1][q] = *(const u32x4*)(kl + ((gi + 1) * 3 + q) * 1024);
      }
#pragma unroll
      for (int q = 0; q < 3; ++q) sc[gi * 3 + q] = mfma16(kf[gi & 1][q], qf, f32x4{0, 0, 0, 0});
      if (gi == 1 || gi == 4) pf_(gi >> 2);
      SB();
    }
  }
  asm volatile("s_nop 15\n\ts_nop 15\n\ts_nop 15" ::: "memory");
  SB();
  const int dq = 16 * (16 + w) + tok - (16 * t0 + 4 * g);
  const float NEG = -INFINITY;
#pragma unroll
  for (int i = 0; i < 18; ++i) {
    if (i < 2 || i >= 16) {
#pragma unroll
      for (int r = 0; r < 4; ++r) sc[i][r] = ((unsigned)(dq - 16 * i - r) < 256u) ? sc[i][r] : NEG;
    }
  }
  if (p < 2) {
    const int nneg = 16 - 8 * p - t0;
#pragma unroll
    for (int i = 0; i < 16; ++i)
      if (i < nneg) sc[i] = f32x4{NEG, NEG, NEG, NEG};
  }
  float m0 = max3f(sc[0][0], sc[0][1], sc[0][2]), m1 = max3f(sc[0][3], sc[1][0], sc[1][1]), m2 = max3f(sc[1][2], sc[1][3], sc[1][3]);
#pragma unroll
  for (int i = 2; i < 17; i += 3) {
    m0 = max3f(m0, sc[i][0], sc[i][1]); m1 = max3f(m1, sc[i][2], sc[i][3]);
    m2 = max3f(m2, sc[i + 1][0], sc[i + 1][1]); m0 = max3f(m0, sc[i + 1][2], sc[i + 1][3]);
    m1 = max3f(m1, sc[i + 2][0], sc[i + 2][1]); m2 = max3f(m2, sc[i + 2][2], sc[i + 2][3]);
  }
  m0 = max3f(m0, sc[17][0], sc[17][1]); m1 = max3f(m1, sc[17][2], sc[17][3]);
  float m = max3f(m0, m1, m2);
  {
    auto pp = __builtin_amdgcn_permlane16_swap(__float_as_uint(m), __float_as_uint(m), false, false);
    m = max3f(__uint_as_float(pp[0]), __uint_as_float(pp[1]), __uint_as_float(pp[1]));
    auto qq = __builtin_amdgcn_permlane32_swap(__float_as_uint(m), __float_as_uint(m), false, false);
    m = max3f(__uint_as_float(qq[0]), __uint_as_float(qq[1]), __uint_as_float(qq[1]));
  }
  const float cs = 0.18898223650461362f * 1.4426950408889634f;
  const float mc = m * cs;
  const f32x2 cs2 = {cs, cs}, mc2 = {-mc, -mc};
  u32x4 pf[9];
#pragma unroll
  for (int i = 0; i < 18; ++i) {
    f32x2 x0 = __builtin_elementwise_fma(f32x2{sc[i][0], sc[i][1]}, cs2, mc2);
    f32x2 x1 = __builtin_elementwise_fma(f32x2{sc[i][2], sc[i][3]}, cs2, mc2);
    pf[i >> 1][(i & 1) * 2 + 0] = pk_bf16(__builtin_amdgcn_exp2f(x0[0]), __builtin_amdgcn_exp2f(x0[1]));
    pf[i >> 1][(i & 1) * 2 + 1] = pk_bf16(__builtin_amdgcn_exp2f(x1[0]), __builtin_amdgcn_exp2f(x1[1]));
    if ((i & 3) == 1 && i < 16) { SB(); pf_(2 + (i >> 2)); SB(); }
  }
  const int q4 = (lane & 15) >> 2, p4 = lane & 3;
  const char* vl = kv + 24 * 1024 + t0 * 1024;
  const int vb = p4 * 256 + (((4 * g + q4) * 16 + (p4 >> 1) * 128) & 255);
  const int vofs[2] = {vb + (p4 & 1) * 8, vb + ((p4 & 1) ^ 1) * 8};
  f32x4 o[2] = {f32x4{0, 0, 0, 0}, f32x4{0, 0, 0, 0}};
  SB();
  {
    u32x4 vf[3][2];
    auto rdv = [&](int bb) {
#pragma unroll
      for (int f = 0; f < 2; ++f) vf[bb % 3][f] = tr_frag(vl + vofs[f] + (2 * bb) * 1024, vl + vofs[f] + (2 * bb + 1) * 1024);
    };
    rdv(0); rdv(1);
#pragma unroll
    for (int bb = 0; bb < 9; ++bb) {
      if (bb + 2 < 9) rdv(bb + 2);
#pragma unroll
      for (int f = 0; f < 2; ++f) o[f] = mfma16(vf[bb % 3][f], pf[bb], o[f]);
      SB();
    }
  }
  const float lsum = __shfl(o[1][0], 48 + tok);
  const float inv = __builtin_amdgcn_rcpf(lsum);
#pragma unroll
  for (int f = 0; f < 2; ++f) {
    if (f == 0 || g < 3) {
      const int k0 = DH * head + 16 * f + 4 * g;
      u32x2 st = {pk_bf16(o[f][0] * inv, o[f][1] * inv), pk_bf16(o[f][2] * inv, o[f][3] * inv)};
      st_sc1_b64(cx, (unsigned)(((k0 >> 5) * 64 + ((k0 & 31) >> 3) * 16 + tok) * 16 + (k0 & 7) * 2), ooff, st);
    }
  }
}

enum { E_OD0, E_OD1, E_OR0, E_OR1, E_P30, E_P31, E_X1D0, E_X1D1, E_X1F0, E_X1R0, E_X1B, E_ST0, E_ST1, E_HD0, E_HD1, E_HR0, E_HR1, E_T4A, E_T4D, E_P5P0, E_P5P1, E_P5F, E_X2D, E_COUNT };
__device__ __forceinline__ unsigned ev_arrive(char* smem, int e, int lane) {
  asm volatile("s_waitcnt lgkmcnt(0)" ::: "memory");
  unsigned old = 0;
  if (lane == 0) old = __hip_atomic_fetch_add((LAS unsigned*)(smem + LDS_EV + e * 4), 1u, __ATOMIC_RELAXED, __HIP_MEMORY_SCOPE_WORKGROUP);
  return __builtin_amdgcn_readfirstlane(old);
}
__device__ __forceinline__ void ev_post(char* smem, int e, int lane) {
  asm volatile("s_waitcnt lgkmcnt(0)" ::: "memory");
  if (lane == 0) __hip_atomic_fetch_add((LAS unsigned*)(smem + LDS_EV + e * 4), 1u, __ATOMIC_RELAXED, __HIP_MEMORY_SCOPE_WORKGROUP);
}
__device__ __forceinline__ unsigned ev_peek(char* smem, int e) {
  unsigned v = __hip_atomic_load((LAS unsigned*)(smem + LDS_EV + e * 4), __ATOMIC_RELAXED, __HIP_MEMORY_SCOPE_WORKGROUP);
  return __builtin_amdgcn_readfirstlane(v);
}
__device__ __forceinline__ void ev_wait(char* smem, int e, unsigned target) {
  while (ev_peek(smem, e) < target) __builtin_amdgcn_s_sleep(1);
  asm volatile("" ::: "memory");
  SB();
}
#define BAR() do { __builtin_amdgcn_s_barrier(); asm volatile("" ::: "memory"); SB(); } while (0)
__device__ __forceinline__ u32x2 add_res(f32x4 y, u32x2 r) {
  const unsigned y0 = pk_bf16(y[0], y[1]), y1 = pk_bf16(y[2], y[3]);
  return u32x2{pk_bf16(bf_lo(r[0]) + bf_lo(y0), bf_hi(r[0]) + bf_hi(y0)), pk_bf16(bf_lo(r[1]) + bf_lo(y1), bf_hi(r[1]) + bf_hi(y1))};
}

__global__ __launch_bounds__(512, 2) void k_main(char* __restrict__ ws_, const char* __restrict__ xin, char* __restrict__ out) {
  extern __shared__ __attribute__((aligned(16))) char smem[];
  Ctx cx;
  cx.ws = ws_;
  cx.rs = __builtin_amdgcn_make_buffer_rsrc(ws_, 0, 0x7fffffff, 0x00020000);
  const __amdgpu_buffer_rsrc_t rx = __builtin_amdgcn_make_buffer_rsrc((void*)xin, 0, S_ * D_ * 2, 0x00020000);
  const __amdgpu_buffer_rsrc_t ro = __builtin_amdgcn_make_buffer_rsrc(out, 0, S_ * D_ * 2, 0x00020000);
  if (threadIdx.x < 64) *(unsigned*)(smem + LDS_EV + threadIdx.x * 4) = 0u;

#pragma unroll 1
  for (int L = 0; L < 2; ++L) {
    int tid = threadIdx.x, b = blockIdx.x;
    asm volatile("" : "+v"(tid));
    asm volatile("" : "+s"(b));
    const int lane = tid & 63, wave = __builtin_amdgcn_readfirstlane(tid >> 6);
    const int xcd = b & 7, rr = b >> 3;
    const int p = MAPMODE == 0 ? xcd * 2 + (rr >> 4) : MAPMODE == 1 ? (xcd & 3) * 4 + (rr >> 3) : (xcd & 1) * 8 + (rr >> 2);
    const int j = MAPMODE == 0 ? (rr & 15) : MAPMODE == 1 ? (xcd >> 2) * 8 + (rr & 7) : (xcd >> 1) * 4 + (rr & 3);
    const int tok = lane & 15, g = lane >> 4;
    const bool projwg = (j & 7) < 7;
    const int qq = j >> 3, nblk = projwg ? (j & 7) : 6;
    const unsigned l16 = lane * 16;
    const unsigned r8t = (unsigned)(((g >> 1) * 16 + tok) * 16 + (g & 1) * 8);
    const unsigned r8r = (unsigned)(tok * (D_ * 2) + g * 8);
    const int tfw = p * 8 + wave;
    const unsigned M1 = (unsigned)(L + 1);
    char* const RW = smem + (L ? 0 : LDS_REG);
    char* const RA = smem + (L ? LDS_REG : 0);
    const __amdgpu_buffer_rsrc_t rsx = L ? cx.rs : rx;
    const unsigned xbase = L ? OFF_X + XT_BYTES : 0u;
    STAMP(0);
    if (L == 0) {
      if (wave < 7) {
#pragma unroll
        for (int i = 0; i < 12; ++i) dma<0>(cx, l16, OFF_W1 + (unsigned)(j * 84 + wave * 12 + i) * 1024, smem + LDS_REG + (wave * 12 + i) * 1024);
      }
      SB();
    } else {
      if (wave == 7) poll16(cx, flag_line(L - 1, F_X2, p, j), PROJ14, lane);
      BAR();
    }
    STAMP(1);
    u32x4 qf;
    {
      u32x4 a[KS];
      const unsigned va = L ? l16 : (unsigned)(tok * (D_ * 2) + g * 16), sst = L ? 1024u : 64u;
#pragma unroll
      for (int s = 0; s < KS; ++s) a[s] = __builtin_amdgcn_raw_buffer_load_b128(rsx, va, xbase + (unsigned)tfw * 14336u + s * sst, 16);
      SB();
      if (L == 0) {
        if (wave == 7) {
          const unsigned o1 = OFF_CS1 + (unsigned)(j * 96) * 4, o4 = OFF_CSGU + (unsigned)(j * 7 * 32) * 4;
          const unsigned lq = lane < 24 ? l16 : 0u, lg = lane < 56 ? l16 : 0u;
          u32x4 q0 = ld_w(cx, lq, o1), q1 = ld_w(cx, lq, o1 + 8192), g0 = ld_w(cx, lg, o4), g1 = ld_w(cx, lg, o4 + 16384);
          if (lane < 24) *(f32x4*)(smem + LDS_TAB + 2048 + l16) = __builtin_bit_cast(f32x4, q0) + __builtin_bit_cast(f32x4, q1);
          if (lane < 56) *(f32x4*)(smem + LDS_TAB + 1024 + l16) = __builtin_bit_cast(f32x4, g0) + __builtin_bit_cast(f32x4, g1);
        } else {
          WAIT_VM(14);
        }
        BAR_LDS();
        SB();
      }
      STAMP(2);
      auto w1tile = [&](int ti) { return *(const u32x4*)((ti < 56 ? RW + ti * 1024 : smem + LDS_W1B + (ti - 56) * 1024) + l16); };
      f32x2 st;
      {
        f32x4 acc[4], gram = {0, 0, 0, 0};
#pragma unroll
        for (int c = 0; c < 4; ++c) acc[c] = f32x4{0, 0, 0, 0};
        u32x4 wb[3][4];
#pragma unroll
        for (int s = 0; s < 2; ++s)
#pragma unroll
          for (int c = 0; c < 4; ++c) wb[s][c] = w1tile(s * 6 + 2 + c);
#pragma unroll
        for (int s = 0; s < KS; ++s) {
          if (s + 2 < KS) {
#pragma unroll
            for (int c = 0; c < 4; ++c) wb[(s + 2) % 3][c] = w1tile((s + 2) * 6 + 2 + c);
          }
          gram = mfma16(a[s], a[s], gram);
#pragma unroll
          for (int c = 0; c < 4; ++c) acc[c] = mfma16(wb[s % 3][c], a[s], acc[c]);
          SB();
        }
        STAMP(3);
        st = stat_finish(red_add_g(g == 3 ? acc[1][0] : 0.f), gram, tok, g);
        const float mi = st[0] * st[1];
        unsigned pk[4][2];
#pragma unroll
        for (int c = 0; c < 4; ++c) {
          const f32x4 csum = *(const f32x4*)(smem + LDS_TAB + 2048 + (c + 2) * 64 + g * 16);
          float v[4];
#pragma unroll
          for (int r = 0; r < 4; ++r) v[r] = fmaf(acc[c][r], st[1], -mi * csum[r]);
          if (c == 1 && g == 3) v[0] = 0.0f;
          if (c == 3 && g == 3) v[0] = 1.0f;
          pk[c][0] = pk_bf16(v[0], v[1]);
          pk[c][1] = pk_bf16(v[2], v[3]);
        }
        u32x4 kf = {pk[0][0], pk[0][1], pk[1][0], pk[1][1]};
        u32x4 vf = (g & 1) ? u32x4{pk[3][0], pk[3][1], pk[2][0], pk[2][1]} : u32x4{pk[2][0], pk[2][1], pk[3][0], pk[3][1]};
        const unsigned vslot = (unsigned)(g * 256 + ((tok * 16 + (g >> 1) * 128) & 255));
        st_sc1(cx, l16, OFF_K + L * KV_BYTES + (unsigned)(j * 128 + tfw) * 1024, kf);
        st_sc1(cx, vslot, OFF_V + L * KV_BYTES + (unsigned)(j * 128 + tfw) * 1024, vf);
        *(u32x4*)(RA + (16 + wave) * 1024 + l16) = kf;
        *(u32x4*)(RA + (24 + 16 + wave) * 1024 + vslot) = vf;
        if (p < 2) {
          u32x4 z = {0, 0, 0, 0};
          asm volatile("" : "+v"(z));
#pragma unroll
          for (int i = 0; i < 2; ++i) {
            const int ti = wave * 2 + i;
            if (p * 8 - 16 + ti < 0) {
              *(u32x4*)(RA + ti * 1024 + l16) = z;
              *(u32x4*)(RA + (24 + ti) * 1024 + l16) = z;
            }
          }
        }
        WAIT_VM(0);
        BAR_LDS();
      }
      STAMP(4);
      if (wave == 7 && lane < 2 && p + 1 + lane < 16)
        __hip_atomic_store((gu32*)(cx.ws + flag_line(L, F_KV, p + 1 + lane, j) + lane * 4), 1u, __ATOMIC_RELAXED, __HIP_MEMORY_SCOPE_AGENT);
      SB();
      {
        f32x4 acc[2] = {f32x4{0, 0, 0, 0}, f32x4{0, 0, 0, 0}};
        u32x4 wb[4][2];
#pragma unroll
        for (int s = 0; s < 3; ++s) { wb[s][0] = w1tile(s * 6); wb[s][1] = w1tile(s * 6 + 1); }
#pragma unroll
        for (int s = 0; s < KS; ++s) {
          if (s + 3 < KS) { wb[(s + 3) & 3][0] = w1tile((s + 3) * 6); wb[(s + 3) & 3][1] = w1tile((s + 3) * 6 + 1); }
          acc[0] = mfma16(wb[s & 3][0], a[s], acc[0]);
          acc[1] = mfma16(wb[s & 3][1], a[s], acc[1]);
          SB();
        }
        const float mi = st[0] * st[1];
#pragma unroll
        for (int c = 0; c < 2; ++c) {
          const f32x4 csum = *(const f32x4*)(smem + LDS_TAB + 2048 + c * 64 + g * 16);
          float v[4];
#pragma unroll
          for (int r = 0; r < 4; ++r) v[r] = fmaf(acc[c][r], st[1], -mi * csum[r]);
          qf[2 * c] = pk_bf16(v[0], v[1]);
          qf[2 * c + 1] = pk_bf16(v[2], v[3]);
        }
      }
    }
    if (wave == 7 && p >= 1) poll16(cx, flag_line(L, F_KV, p, j), p >= 2 ? 3u : 1u, lane);
    const int Fi = j * 7 + (wave < 7 ? wave : 0);
    constexpr int W4A = 6, W4E = 10;
    u32x4 w4[KS][2];
    BAR();
#pragma unroll
    for (int i = 0; i < 2; ++i) {
      const int ti = wave * 2 + i, T = p * 8 - 16 + ti;
      if (T >= 0) {
        dma<16>(cx, l16, OFF_K + L * KV_BYTES + (unsigned)(j * 128 + T) * 1024, RA + ti * 1024);
        dma<16>(cx, l16, OFF_V + L * KV_BYTES + (unsigned)(j * 128 + T) * 1024, RA + (24 + ti) * 1024);
      }
    }
    WAIT_VM(0);
    BAR();
    STAMP(5);
    const int h = wave >> 2, wl = wave & 3;
    if (projwg && h == 0) {
#pragma unroll
      for (int i = 0; i < 14; ++i) dma<0>(cx, l16, OFF_WO + (unsigned)(nblk * 56 + wl * 14 + i) * 1024, RW + (wl * 14 + i) * 1024);
    }
    SB();
    if (h == 0) __builtin_amdgcn_s_setprio(2);
    attn_body(cx, RA, qf, wave, lane, p, OFF_O + L * XT_BYTES + (unsigned)(tfw * KS) * 1024, j, [&](int s) {
      w4[s][0] = ld_w(cx, l16, OFF_WGU + (unsigned)((Fi * KS + s) * 2) * 1024);
      w4[s][1] = ld_w(cx, l16, OFF_WGU + (unsigned)((Fi * KS + s) * 2 + 1) * 1024);
    });
    __builtin_amdgcn_s_setprio(0);
    STAMP(6);
    WAIT_VM(0);
    if (ev_arrive(smem, E_OD0 + h, lane) == 4 * M1 - 1) signal16(cx, L, F_OA + h, p, j, lane);
    STAMP(7);
    const bool poller = wave == 0 || wave == 7;
    if (poller) {
      if (projwg) { poll16(cx, flag_line(L, F_OA + h, p, j), ALL16, lane); ev_post(smem, E_OR0 + h, lane); }
#pragma unroll
      for (int s = W4A; s < W4E; ++s) { w4[s][0] = u32x4{0, 0, 0, 0}; w4[s][1] = u32x4{0, 0, 0, 0}; }
    } else {
#pragma unroll
      for (int s = W4A; s < W4E; ++s) {
        w4[s][0] = ld_w(cx, l16, OFF_WGU + (unsigned)((Fi * KS + s) * 2) * 1024);
        w4[s][1] = ld_w(cx, l16, OFF_WGU + (unsigned)((Fi * KS + s) * 2 + 1) * 1024);
      }
    }
    SB();
    if (projwg) {
      const int t3 = wl & 1, kh = wl >> 1;
      const int tf3 = p * 8 + h * 4 + qq * 2 + t3;
      const char* wlp = RW + kh * 28 * 1024 + l16;
      ev_wait(smem, E_OD0, 4 * M1);
      u32x4 wf[3][4];
#pragma unroll
      for (int i = 0; i < 2; ++i)
#pragma unroll
        for (int jj = 0; jj < 4; ++jj) wf[i][jj] = *(const u32x4*)(wlp + (i * 4 + jj) * 1024);
      u32x2 rs[2];
#pragma unroll
      for (int q = 0; q < 2; ++q) {
        const int jj = kh * 2 + q;
        const unsigned so = L ? (unsigned)(tf3 * KS + 2 * nblk + kh) * 1024 + q * 512 : (unsigned)tf3 * 14336u + nblk * 128 + jj * 32;
        rs[q] = __builtin_amdgcn_raw_buffer_load_b64(rsx, L ? r8t : r8r, xbase + so, 16);
      }
      ev_wait(smem, E_OR0 + h, M1);
      STAMP(8);
      u32x4 a3[7];
#pragma unroll
      for (int i = 0; i < 7; ++i) a3[i] = ld_sc1(cx, l16, OFF_O + L * XT_BYTES + (unsigned)(tf3 * KS + kh * 7 + i) * 1024);
      SB();
      f32x4 acc[4];
#pragma unroll
      for (int jj = 0; jj < 4; ++jj) acc[jj] = f32x4{0, 0, 0, 0};
#pragma unroll
      for (int i = 0; i < 7; ++i) {
        if (i + 2 < 7) {
#pragma unroll
          for (int jj = 0; jj < 4; ++jj) wf[(i + 2) % 3][jj] = *(const u32x4*)(wlp + ((i + 2) * 4 + jj) * 1024);
        }
#pragma unroll
        for (int jj = 0; jj < 4; ++jj) acc[jj] = mfma16(wf[i % 3][jj], a3[i], acc[jj]);
        SB();
      }
      char* pp3 = smem + LDS_P3 + l16;
      *(f32x4*)(pp3 + (wave * 2 + 0) * 1024) = kh ? acc[0] : acc[2];
      *(f32x4*)(pp3 + (wave * 2 + 1) * 1024) = kh ? acc[1] : acc[3];
      ev_post(smem, E_P30 + h, lane);
      ev_wait(smem, E_P30 + h, 4 * M1);
#pragma unroll
      for (int q = 0; q < 2; ++q) {
        f32x4 y = (kh ? acc[2 + q] : acc[q]) + *(const f32x4*)(pp3 + ((wave ^ 2) * 2 + q) * 1024);
        st_sc1_b64(cx, r8t, OFF_X1 + L * XT_BYTES + (unsigned)(tf3 * KS + 2 * nblk + kh) * 1024 + q * 512, add_res(y, rs[q]));
      }
      WAIT_VM(0);
      if (ev_arrive(smem, E_X1D0 + h, lane) == 4 * M1 - 1) signal16(cx, L, F_X1A + h, p, j, lane);
    }
    STAMP(9);
    u32x4 w5[8][4];
    const unsigned w5base = OFF_WD + (unsigned)((nblk * 56 + wave * 8) * 4) * 1024;
    if (wave == 0) {
#pragma unroll
      for (int s = W4A; s < W4E; ++s) {
        w4[s][0] = ld_w(cx, l16, OFF_WGU + (unsigned)((Fi * KS + s) * 2) * 1024);
        w4[s][1] = ld_w(cx, l16, OFF_WGU + (unsigned)((Fi * KS + s) * 2 + 1) * 1024);
      }
    }
    if (wave < 7) {
#pragma unroll
      for (int s = W4E; s < KS; ++s) {
        w4[s][0] = ld_w(cx, l16, OFF_WGU + (unsigned)((Fi * KS + s) * 2) * 1024);
        w4[s][1] = ld_w(cx, l16, OFF_WGU + (unsigned)((Fi * KS + s) * 2 + 1) * 1024);
      }
    } else {
#pragma unroll
      for (int s = W4E; s < KS; ++s) { w4[s][0] = u32x4{0, 0, 0, 0}; w4[s][1] = u32x4{0, 0, 0, 0}; }
    }
    SB();
    if (h == 0) {
      if (wave == 0) {
        poll16(cx, flag_line(L, F_X1A, p, j), PROJ14, lane);
        ev_wait(smem, E_OD1, 4 * M1);
        ev_post(smem, E_X1F0, lane);
      }
      ev_wait(smem, E_X1F0, M1);
      STAMP(10);
#pragma unroll
      for (int s = 0; s < KS; ++s) dma<16>(cx, l16, OFF_X1 + L * XT_BYTES + (unsigned)(tfw * KS + s) * 1024, RA + (wl * KS + s) * 1024);
      WAIT_VM(0);
      ev_post(smem, E_X1R0, lane);
      const u32x4 ones = {0x3f803f80u, 0x3f803f80u, 0x3f803f80u, 0x3f803f80u};
      f32x4 sm = {0, 0, 0, 0}, gr = {0, 0, 0, 0};
#pragma unroll
      for (int hf = 0; hf < 2; ++hf) {
        u32x4 xs[7];
#pragma unroll
        for (int s = 0; s < 7; ++s) xs[s] = *(const u32x4*)(RA + (wl * KS + hf * 7 + s) * 1024 + l16);
#pragma unroll
        for (int s = 0; s < 7; ++s) { sm = mfma16(ones, xs[s], sm); gr = mfma16(xs[s], xs[s], gr); }
        SB();
      }
      const f32x2 st = stat_finish(sm[0], gr, tok, g);
      if (g == 0) *(f32x2*)(smem + LDS_TAB + (wave * 16 + tok) * 8) = f32x2{st[0], st[1] * st[1]};
      ev_post(smem, E_ST0, lane);
    }
    if (wave < 7) ev_wait(smem, E_X1R0, 4 * M1);
    STAMP(11);
    const int tf5 = p * 8 + qq * 2;
    const int ft = wave >> 2, fj = wave & 3;
    const unsigned fso = (unsigned)(2 * nblk + (fj >> 1)) * 1024 + (fj & 1) * 512;
    u32x4 hb[8][2];
    bool hb_issued = false;
    if (wave < 7) {
#pragma unroll
      for (int hh = 0; hh < 2; ++hh) {
        const char* xt = (hh == 0 ? RA : RW) + l16;
        f32x4 acc[4][2];
#pragma unroll
        for (int t = 0; t < 4; ++t) { acc[t][0] = f32x4{0, 0, 0, 0}; acc[t][1] = f32x4{0, 0, 0, 0}; }
        u32x4 xb[3][4];
        auto rd = [&](int s) {
#pragma unroll
          for (int t = 0; t < 4; ++t) xb[s % 3][t] = *(const u32x4*)(xt + (t * KS + s) * 1024);
        };
        rd(0); rd(1);
        SB();
#pragma unroll
        for (int s = 0; s < KS; ++s) {
          if (s + 2 < KS) rd(s + 2);
#pragma unroll
          for (int t = 0; t < 4; ++t) {
            acc[t][0] = mfma16(w4[s][0], xb[s % 3][t], acc[t][0]);
            acc[t][1] = mfma16(w4[s][1], xb[s % 3][t], acc[t][1]);
          }
          SB();
          if (hh == 0 && s >= 6) { w5[(s - 6) >> 2][(s - 6) & 3] = ld_w(cx, l16, w5base + (s - 6) * 1024); SB(); }
          if (hh == 1 && s < 12) {
#pragma unroll
            for (int q = 0; q < 2; ++q) { const int idx = 8 + s * 2 + q; w5[idx >> 2][idx & 3] = ld_w(cx, l16, w5base + idx * 1024); }
            SB();
          }
          if (hh == 1 && s == 2) {
            WAIT_VM(6);
            if (ev_arrive(smem, E_HD0, lane) == 7 * M1 - 1) signal16(cx, L, F_HA, p, j, lane);
            SB();
          }
        }
        STAMP(12 + hh * 2);
        ev_post(smem, E_T4A + hh, lane);
        ev_wait(smem, E_ST0 + hh, hh == 0 ? 4 * M1 : M1);
        const f32x4 cg = *(const f32x4*)(smem + LDS_TAB + 1024 + wave * 128 + g * 16);
        const f32x4 cu = *(const f32x4*)(smem + LDS_TAB + 1024 + wave * 128 + 64 + g * 16);
        unsigned hv[4][2];
#pragma unroll
        for (int t = 0; t < 4; ++t) {
          const f32x2 s2 = *(const f32x2*)(smem + LDS_TAB + ((hh * 4 + t) * 16 + tok) * 8);
          float hq[4];
          float nm = -s2[0], i2 = s2[1];
#pragma unroll
          for (int r = 0; r < 4; ++r) {
            float tg = fmaf(nm, cg[r], acc[t][0][r]), tu = fmaf(nm, cu[r], acc[t][1][r]);
            asm volatile("" : "+v"(tg), "+v"(tu));
            tg *= i2;
            asm volatile("" : "+v"(tg));
            hq[r] = tg * tu;
          }
          hv[t][0] = pk_bf16(hq[0], hq[1]);
          hv[t][1] = pk_bf16(hq[2], hq[3]);
        }
        const unsigned hlane = (unsigned)((g & 1) * (KSF * 1024) + ((Fi & 1) * 2 + (g >> 1)) * 256 + tok * 16);
#pragma unroll
        for (int u = 0; u < 2; ++u) {
          auto s0 = __builtin_amdgcn_permlane16_swap(hv[2 * u][0], hv[2 * u + 1][0], false, false);
          auto s1 = __builtin_amdgcn_permlane16_swap(hv[2 * u][1], hv[2 * u + 1][1], false, false);
          u32x4 o = {s0[0], s1[0], s0[1], s1[1]};
          st_sc1(cx, hlane, OFF_H + L * H_BYTES + (unsigned)((p * 8 + hh * 4 + 2 * u) * KSF + (Fi >> 1)) * 1024, o);
        }
        SB();
        if (hh == 0) { ev_wait(smem, E_X1B, M1); STAMP(13); }
      }
      hb_issued = projwg && ev_peek(smem, E_HR0) >= M1;
      asm volatile("" ::: "memory");
      if (hb_issued) {
        const unsigned hbase = OFF_H + L * H_BYTES + (unsigned)(tf5 * KSF + wave * 8) * 1024;
#pragma unroll
        for (int i = 0; i < 8; ++i)
#pragma unroll
          for (int t = 0; t < 2; ++t) hb[i][t] = ld_sc1(cx, l16, hbase + (unsigned)(t * KSF + i) * 1024);
        SB();
        WAIT_VM(16);
      } else {
#pragma unroll
        for (int i = 0; i < 8; ++i) { hb[i][0] = u32x4{0, 0, 0, 0}; hb[i][1] = u32x4{0, 0, 0, 0}; }
        WAIT_VM(0);
      }
      if (ev_arrive(smem, E_HD1, lane) == 7 * M1 - 1) signal16(cx, L, F_HB, p, j, lane);
      STAMP(15);
    } else {
#pragma unroll
      for (int i = 0; i < 32; ++i) w5[i >> 2][i & 3] = u32x4{0, 0, 0, 0};
#pragma unroll
      for (int i = 0; i < 8; ++i) { hb[i][0] = u32x4{0, 0, 0, 0}; hb[i][1] = u32x4{0, 0, 0, 0}; }
      const u32x4 ones = {0x3f803f80u, 0x3f803f80u, 0x3f803f80u, 0x3f803f80u};
#pragma unroll
      for (int hh = 1; hh < 2; ++hh) {
        poll16(cx, flag_line(L, F_X1B, p, j), PROJ14, lane);
        if (projwg) { ev_wait(smem, E_X1D0, 4 * M1); ev_wait(smem, E_X1D1, 4 * M1); }
        else ev_wait(smem, E_OD0, 4 * M1);
#pragma unroll 1
        for (int i = 0; i < 56; i += 8) {
#pragma unroll
          for (int q = 0; q < 8; ++q) dma<16>(cx, l16, OFF_X1 + L * XT_BYTES + (unsigned)((p * 8 + 4) * KS + i + q) * 1024, RW + (i + q) * 1024);
        }
        WAIT_VM(0);
        ev_post(smem, E_X1B, lane);
        SB();
        const char* xt = (hh == 0 ? RA : RW) + l16;
        f32x4 sm[4], gr[4];
#pragma unroll
        for (int t = 0; t < 4; ++t) { sm[t] = f32x4{0, 0, 0, 0}; gr[t] = f32x4{0, 0, 0, 0}; }
        u32x4 xb[3][4];
        auto rd = [&](int s) {
#pragma unroll
          for (int t = 0; t < 4; ++t) xb[s % 3][t] = *(const u32x4*)(xt + (t * KS + s) * 1024);
        };
        rd(0); rd(1);
        SB();
#pragma unroll
        for (int s = 0; s < KS; ++s) {
          if (s + 2 < KS) rd(s + 2);
#pragma unroll
          for (int t = 0; t < 4; ++t) {
            sm[t] = mfma16(ones, xb[s % 3][t], sm[t]);
            gr[t] = mfma16(xb[s % 3][t], xb[s % 3][t], gr[t]);
          }
          SB();
        }
#pragma unroll
        for (int t = 0; t < 4; ++t) {
          const f32x2 st = stat_finish(sm[t][0], gr[t], tok, g);
          if (g == 0) *(f32x2*)(smem + LDS_TAB + ((hh * 4 + t) * 16 + tok) * 8) = f32x2{st[0], st[1] * st[1]};
        }
        ev_post(smem, E_ST0 + hh, lane);
        STAMP(12 + hh);
      }
      if (L == 0) {
        ev_wait(smem, E_T4A, 7 * M1);
#pragma unroll 1
        for (int i = 0; i < 84; i += 4) {
#pragma unroll
          for (int q = 0; q < 4; ++q)
            dma<0>(cx, l16, OFF_W1 + (unsigned)(j * 84 + i + q) * 1024, i < 56 ? RA + (i + q) * 1024 : smem + LDS_W1B + (i - 56 + q) * 1024);
        }
        SB();
      }
      STAMP(14);
      if (projwg) {
        poll16(cx, flag_line(L, F_HA, p, j), ALL16, lane);
        ev_post(smem, E_HR0, lane);
        STAMP(15);
        poll16(cx, flag_line(L, F_HB, p, j), ALL16, lane);
        ev_post(smem, E_HR1, lane);
        STAMP(16);
      }
    }
    if (projwg) {
      u32x2 r5[2];
#pragma unroll
      for (int hh = 0; hh < 2; ++hh)
        r5[hh] = __builtin_amdgcn_raw_buffer_load_b64(cx.rs, r8t, OFF_X1 + L * XT_BYTES + (unsigned)((tf5 + hh * 4 + ft) * KS) * 1024 + fso, 16);
#pragma unroll
      for (int hh = 0; hh < 2; ++hh) {
        if (wave < 7) {
          if (!hb_issued) {
            ev_wait(smem, E_HR0 + hh, M1);
            const unsigned hbase = OFF_H + L * H_BYTES + (unsigned)((tf5 + hh * 4) * KSF + wave * 8) * 1024;
#pragma unroll
            for (int i = 0; i < 8; ++i)
#pragma unroll
              for (int t = 0; t < 2; ++t) hb[i][t] = ld_sc1(cx, l16, hbase + (unsigned)(t * KSF + i) * 1024);
          }
          STAMP(16 + hh * 3);
          SB();
          f32x4 acc[4][2];
#pragma unroll
          for (int jj = 0; jj < 4; ++jj) { acc[jj][0] = f32x4{0, 0, 0, 0}; acc[jj][1] = f32x4{0, 0, 0, 0}; }
#pragma unroll
          for (int i = 0; i < 8; ++i)
#pragma unroll
            for (int jj = 0; jj < 4; ++jj)
#pragma unroll
              for (int t = 0; t < 2; ++t) acc[jj][t] = mfma16(w5[i][jj], hb[i][t], acc[jj][t]);
          STAMP(17 + hh * 3);
          if (hh == 0) { ev_wait(smem, E_T4D, 7 * M1); ev_wait(smem, E_ST1, M1); } else ev_wait(smem, E_P5F, 8 * M1);
#pragma unroll
          for (int t = 0; t < 2; ++t)
#pragma unroll
            for (int jj = 0; jj < 4; ++jj) *(f32x4*)(RW + (wave * 8 + t * 4 + jj) * 1024 + l16) = acc[jj][t];
          ev_post(smem, E_P5P0 + hh, lane);
          if (hh == 0) {
            ev_wait(smem, E_HR1, M1);
            hb_issued = true;
            {
              const unsigned hbase = OFF_H + L * H_BYTES + (unsigned)((tf5 + 4) * KSF + wave * 8) * 1024;
#pragma unroll
              for (int i = 0; i < 8; ++i)
#pragma unroll
                for (int t = 0; t < 2; ++t) hb[i][t] = ld_sc1(cx, l16, hbase + (unsigned)(t * KSF + i) * 1024);
            }
            SB();
          }
        }
        ev_wait(smem, E_P5P0 + hh, 7 * M1);
        STAMP(18 + hh * 3);
        f32x4 y = *(const f32x4*)(RW + wave * 1024 + l16);
#pragma unroll
        for (int sw = 1; sw < 7; ++sw) y += *(const f32x4*)(RW + (sw * 8 + wave) * 1024 + l16);
        if (hh == 0) ev_post(smem, E_P5F, lane);
        const u32x2 o = add_res(y, r5[hh]);
        const int tf = tf5 + hh * 4 + ft;
        if (L == 0) st_sc1_b64(cx, r8t, OFF_X + XT_BYTES + (unsigned)(tf * KS) * 1024 + fso, o);
        else __builtin_amdgcn_raw_buffer_store_b64(o, ro, r8r, (unsigned)tf * 14336u + nblk * 128 + fj * 32, 0);
      }
      if (L == 0) {
        WAIT_VM(0);
        if (ev_arrive(smem, E_X2D, lane) == 7) signal16(cx, L, F_X2, p, j, lane);
      }
      STAMP(22);
    }
    WAIT_VM(0);
  }
}

extern "C" void kernel_launch(void* const* d_in, const int* in_sizes, int n_in, void* d_out, int out_size, void* d_ws,
                              size_t ws_size, hipStream_t stream) {
  const uint16_t* x = (const uint16_t*)d_in[0];
  const uint16_t* wqkv = (const uint16_t*)d_in[1];
  const uint16_t* wo = (const uint16_t*)d_in[2];
  const uint16_t* wg = (const uint16_t*)d_in[4];
  const uint16_t* wu = (const uint16_t*)d_in[5];
  const uint16_t* wd = (const uint16_t*)d_in[6];
  char* ws = (char*)d_ws;
  constexpr int LP = 224 * 192;
  (void)hipFuncSetAttribute((const void*)k_main, hipFuncAttributeMaxDynamicSharedMemorySize, LDS_TOTAL);
  k_prep<<<222, 512, LP, stream>>>(x, wqkv, wo, wg, wu, wd, ws);
  k_main<<<256, 512, LDS_TOTAL, stream>>>(ws, (const char*)x, (char*)d_out);
}
```

```cpp
#include <hip/hip_runtime.h>
#include <hip/hip_bf16.h>
#include <stdint.h>

typedef __attribute__((ext_vector_type(8))) short s16x8;
typedef __attribute__((ext_vector_type(4))) short s16x4;
typedef __attribute__((ext_vector_type(4))) float f32x4;
typedef __attribute__((ext_vector_type(2))) float f32x2;
typedef __attribute__((ext_vector_type(2))) __bf16 bf16x2;
typedef __attribute__((ext_vector_type(4))) unsigned u32x4;
typedef __attribute__((ext_vector_type(2))) unsigned u32x2;
#define LAS __attribute__((address_space(3)))
typedef __attribute__((address_space(1))) unsigned gu32;

static constexpr int S_ = 2048, D_ = 448, F_ = 1792, NH = 16, DH = 28;
static constexpr int KS = 14, KSF = 56;

static constexpr unsigned OFF_CNT = 0;
static constexpr unsigned OFF_TMO = 196608;
static constexpr unsigned FLAG_BYTES = 196608 + 1024;
static constexpr unsigned OFF_W1 = 262144;
static constexpr unsigned OFF_CS1 = OFF_W1 + 16 * 14 * 6 * 1024;
static constexpr unsigned OFF_WO = OFF_CS1 + 16384;
static constexpr unsigned OFF_WGU = OFF_WO + 7 * 14 * 4 * 1024;
static constexpr unsigned OFF_CSGU = OFF_WGU + 112 * 14 * 2 * 1024;
static constexpr unsigned OFF_WD = OFF_CSGU + 32768;
static constexpr unsigned XT_BYTES = 128 * 14 * 1024;
static constexpr unsigned OFF_X = OFF_WD + 7 * 56 * 4 * 1024;
static constexpr unsigned OFF_X1 = OFF_X + 2 * XT_BYTES;
static constexpr unsigned OFF_O = OFF_X1 + 2 * XT_BYTES;
static constexpr unsigned KV_BYTES = 16 * 128 * 1024;
static constexpr unsigned OFF_K = OFF_O + 2 * XT_BYTES;
static constexpr unsigned OFF_V = OFF_K + 2 * KV_BYTES;
static constexpr unsigned H_BYTES = 128 * 56 * 1024;
static constexpr unsigned OFF_H = OFF_V + 2 * KV_BYTES;

__device__ __forceinline__ float bf_lo(unsigned u) { return __uint_as_float(u << 16); }
__device__ __forceinline__ float bf_hi(unsigned u) { return __uint_as_float(u & 0xffff0000u); }
__device__ __forceinline__ unsigned pk_bf16(float a, float b) {
  f32x2 f = {a, b};
  bf16x2 r = __builtin_convertvector(f, bf16x2);
  return __builtin_bit_cast(unsigned, r);
}
__device__ __forceinline__ float rnd_bf16(float a) { return bf_lo(pk_bf16(a, 0.f)); }
__device__ __forceinline__ float red_add_g(float v) {
  auto p = __builtin_amdgcn_permlane16_swap(__float_as_uint(v), __float_as_uint(v), false, false);
  v = __uint_as_float(p[0]) + __uint_as_float(p[1]);
  auto q = __builtin_amdgcn_permlane32_swap(__float_as_uint(v), __float_as_uint(v), false, false);
  return __uint_as_float(q[0]) + __uint_as_float(q[1]);
}
__device__ __forceinline__ f32x4 mfma16(u32x4 a, u32x4 b, f32x4 c) {
  return __builtin_amdgcn_mfma_f32_16x16x32_bf16(__builtin_bit_cast(s16x8, a), __builtin_bit_cast(s16x8, b), c, 0, 0, 0);
}
__device__ __forceinline__ u32x4 tr_frag(const char* p0, const char* p1) {
  s16x4 lo = __builtin_amdgcn_ds_read_tr16_b64_v4i16((LAS s16x4*)p0);
  s16x4 hi = __builtin_amdgcn_ds_read_tr16_b64_v4i16((LAS s16x4*)p1);
  u32x2 l2 = __builtin_bit_cast(u32x2, lo), h2 = __builtin_bit_cast(u32x2, hi);
  return u32x4{l2[0], l2[1], h2[0], h2[1]};
}
#define SB() __builtin_amdgcn_sched_barrier(0)
#define WAIT_VM(n) asm volatile("s_waitcnt vmcnt(" #n ")" ::: "memory")
#define BAR_LDS() do { asm volatile("s_waitcnt lgkmcnt(0)" ::: "memory"); __builtin_amdgcn_s_barrier(); asm volatile("" ::: "memory"); } while (0)

__device__ __forceinline__ f32x2 stat_finish(float sum, f32x4 gram, int tok, int g) {
  float d = (tok & 2) ? ((tok & 1) ? gram[3] : gram[2]) : ((tok & 1) ? gram[1] : gram[0]);
  d = (g == (tok >> 2)) ? d : 0.f;
  const float sq = red_add_g(d);
  const float mean = sum * (1.0f / D_);
  const float var = fmaxf(fmaf(-mean, mean, sq * (1.0f / D_)), 0.f);
  const float mb = rnd_bf16(mean);
  const float den = rnd_bf16(__builtin_amdgcn_sqrtf(rnd_bf16(rnd_bf16(var) + 1e-5f)));
  return f32x2{mb, __builtin_amdgcn_rcpf(den)};
}

__device__ __forceinline__ void tile_block(const uint16_t* __restrict__ src, int ld, int col0, const int (&rows)[8], char* dst,
                                           int jjstride, int lane, u32x4 (&o)[4], bool consec = false) {
  const int i = lane & 15;
  u32x2 v[8];
#pragma unroll
  for (int e = 0; e < 8; ++e) v[e] = *(const u32x2*)(src + (size_t)rows[e] * ld + col0 + 4 * i);
#pragma unroll
  for (int jj = 0; jj < 4; ++jj) {
#pragma unroll
    for (int d = 0; d < 4; ++d)
      o[jj][d] = __builtin_amdgcn_perm(v[2 * d + 1][jj >> 1], v[2 * d][jj >> 1], (jj & 1) ? 0x07060302u : 0x05040100u);
    if (consec) *(u32x4*)(dst + (size_t)(i >> 2) * jjstride + ((lane >> 4) * 16 + 4 * (i & 3) + jj) * 16) = o[jj];
    else *(u32x4*)(dst + (size_t)jj * jjstride + lane * 16) = o[jj];
  }
}

__global__ __launch_bounds__(512, 2) void k_prep(const uint16_t* __restrict__ x, const uint16_t* __restrict__ wqkv,
                                                 const uint16_t* __restrict__ wo, const uint16_t* __restrict__ wg,
                                                 const uint16_t* __restrict__ wu, const uint16_t* __restrict__ wd,
                                                 char* __restrict__ ws) {
  extern __shared__ __attribute__((aligned(16))) char smem[];
  const int tid = threadIdx.x, lane = tid & 63, wave = __builtin_amdgcn_readfirstlane(tid >> 6);
  const int b = blockIdx.x;
  const int g = lane >> 4;
  {
    const unsigned xo = (unsigned)(b * 512 + tid) * 64u;
    if (wave == 7 || true) { if (xo < (unsigned)(S_ * D_ * 2)) { unsigned t = *(const volatile unsigned*)((const char*)x + xo); asm volatile("" :: "v"(t)); } }
  }
  const u32x4 ones = {0x3f803f80u, 0x3f803f80u, 0x3f803f80u, 0x3f803f80u};
  if (b < 112) {
    const int kh = b & 1, which = (b >> 1) & 1, blk = b >> 2;
    if (wave < 7) {
      const int s = kh * 7 + wave;
      int rows[8];
#pragma unroll
      for (int e = 0; e < 8; ++e) rows[e] = 32 * s + 8 * g + e;
      u32x4 o[4];
      tile_block(which ? wu : wg, F_, blk * 64, rows, ws + OFF_WGU + (size_t)(((blk * 4) * 14 + s) * 2 + which) * 1024,
                 14 * 2 * 1024, lane, o);
#pragma unroll
      for (int jj = 0; jj < 4; ++jj) {
        f32x4 cs = mfma16(o[jj], ones, f32x4{0, 0, 0, 0});
        if ((lane & 15) == 0) *(f32x4*)(smem + ((wave * 4 + jj) * 4 + g) * 16) = cs;
      }
    }
    __syncthreads();
    if (tid < 64) {
      float s = 0.f;
#pragma unroll
      for (int w = 0; w < 7; ++w) s += *(const float*)(smem + (w * 64 + tid) * 4);
      const int jj = tid >> 4, row = tid & 15;
      *(float*)(ws + OFF_CSGU + (size_t)kh * 16384 + (size_t)(((blk * 4 + jj) * 2 + which) * 16 + row) * 4) = s;
    }
  } else if (b < 126) {
    const int t = b - 112, nblk = t >> 1;
    if (wave < 7) {
      const int s = (t & 1) * 7 + wave;
      int rows[8];
#pragma unroll
      for (int e = 0; e < 8; ++e) rows[e] = 32 * s + 8 * g + e;
      u32x4 o[4];
      tile_block(wo, D_, nblk * 64, rows, ws + OFF_WO + (size_t)((nblk * 14 + s) * 4) * 1024, 1024, lane, o, true);
    }
  } else if (b < 182) {
    const int t = b - 126, nblk = t >> 3;
    if (wave < 7) {
      const int s = (t & 7) * 7 + wave;
      const int Fi = 2 * s + (g >> 1);
      int rows[8];
#pragma unroll
      for (int e = 0; e < 8; ++e) rows[e] = 64 * (Fi >> 2) + 16 * (2 * (g & 1) + (e >> 2)) + 4 * (e & 3) + (Fi & 3);
      u32x4 o[4];
      tile_block(wd, D_, nblk * 64, rows, ws + OFF_WD + (size_t)((nblk * 56 + s) * 4) * 1024, 1024, lane, o, true);
    }
  } else if (b < 214) {
    const int head = (b - 182) >> 1, kh = (b - 182) & 1;
    u32x2 v[11];
#pragma unroll
    for (int it = 0; it < 11; ++it) {
      const int c = it * 512 + tid;
      const int row = c / 24, grp = c - row * 24;
      const int part = grp >> 3, gi = grp & 7;
      v[it] = u32x2{grp == 15 ? 0x00003f80u : 0u, 0u};
      if (gi < 7 && row < 224) v[it] = *(const u32x2*)(wqkv + (size_t)(kh * 224 + row) * (3 * D_) + part * D_ + head * DH + gi * 4);
    }
#pragma unroll
    for (int it = 0; it < 11; ++it) {
      const int c = it * 512 + tid;
      const int row = c / 24, grp = c - row * 24;
      int pp = (grp >> 2) + ((row >> 3) & 1);
      if (pp >= 6) pp -= 6;
      if (row < 224) *(u32x2*)(smem + row * 192 + pp * 32 + (grp & 3) * 8) = v[it];
    }
    __syncthreads();
    if (wave < 6) {
      const int c = wave;
      const int q4 = (lane & 15) >> 2, p4 = lane & 3;
      int pp = c + (g & 1);
      if (pp >= 6) pp -= 6;
      const char* base = smem + (8 * g + q4) * 192 + pp * 32 + p4 * 8;
      f32x4 cs = {0, 0, 0, 0};
#pragma unroll
      for (int s = 0; s < 7; ++s) {
        u32x4 wf = tr_frag(base + s * 32 * 192, base + s * 32 * 192 + 4 * 192);
        *(u32x4*)(ws + OFF_W1 + (size_t)((head * 14 + kh * 7 + s) * 6 + c) * 1024 + lane * 16) = wf;
        cs = mfma16(wf, ones, cs);
      }
      if ((lane & 15) == 0) *(f32x4*)(ws + OFF_CS1 + (size_t)kh * 8192 + (size_t)(head * 96 + c * 16 + 4 * g) * 4) = cs;
    }
  } else {
#pragma unroll 1
    for (unsigned o = ((b - 214) * 512 + tid) * 16; o < FLAG_BYTES; o += 8 * 512 * 16) *(u32x4*)(ws + o) = u32x4{0, 0, 0, 0};
  }
}

#define MAPMODE 2
static constexpr int LDS_REG = 57344;
static constexpr int LDS_W1B = 114688;
static constexpr int LDS_P3 = 143360;
static constexpr int LDS_TAB = 159744;
static constexpr int LDS_EV = 162816;
static constexpr int LDS_TOTAL = 163840;

struct Ctx {
  __amdgpu_buffer_rsrc_t rs;
  char* ws;
};
__device__ __forceinline__ u32x4 ld_sc1(const Ctx& c, unsigned voff, unsigned soff) { return __builtin_amdgcn_raw_buffer_load_b128(c.rs, voff, soff, 16); }
__device__ __forceinline__ u32x4 ld_w(const Ctx& c, unsigned voff, unsigned soff) { return __builtin_amdgcn_raw_buffer_load_b128(c.rs, voff, soff, 0); }
__device__ __forceinline__ void st_sc1(const Ctx& c, unsigned voff, unsigned soff, u32x4 v) { __builtin_amdgcn_raw_buffer_store_b128(v, c.rs, voff, soff, 16); }
__device__ __forceinline__ void st_sc1_b64(const Ctx& c, unsigned voff, unsigned soff, u32x2 v) { __builtin_amdgcn_raw_buffer_store_b64(v, c.rs, voff, soff, 16); }
template <int AUX>
__device__ __forceinline__ void dma(const Ctx& c, unsigned voff, unsigned soff, char* lds_uniform) {
  const unsigned la = __builtin_amdgcn_readfirstlane((unsigned)(uintptr_t)(LAS char*)lds_uniform);
  const unsigned so = __builtin_amdgcn_readfirstlane(soff);
  if (AUX) asm volatile("s_mov_b32 m0, %0\n\ts_nop 0\n\tbuffer_load_dwordx4 %1, %2, %3 offen sc1 lds" :: "s"(la), "v"(voff), "s"(c.rs), "s"(so) : "memory");
  else asm volatile("s_mov_b32 m0, %0\n\ts_nop 0\n\tbuffer_load_dwordx4 %1, %2, %3 offen lds" :: "s"(la), "v"(voff), "s"(c.rs), "s"(so) : "memory");
}
__device__ __forceinline__ unsigned flag_line(int L, int e, int p, int c) { return OFF_CNT + (unsigned)((((L * 6 + e) * 16 + p) * 16 + c) * 64); }
__device__ __forceinline__ void signal16(const Ctx& c, int L, int e, int p, int j, int lane) {
  if (lane < 16) __hip_atomic_store((gu32*)(c.ws + flag_line(L, e, p, lane) + j * 4), 1u, __ATOMIC_RELAXED, __HIP_MEMORY_SCOPE_AGENT);
}
__device__ __forceinline__ void poll16(const Ctx& c, unsigned line, unsigned need, int lane) {
  gu32* f = (gu32*)(c.ws + line + (lane & 15) * 4);
  gu32* tmo = (gu32*)(c.ws + OFF_TMO);
  const bool skip = ((need >> (lane & 15)) & 1u) == 0u;
  unsigned spins = 0;
  for (;;) {
    const unsigned v = __hip_atomic_load(f, __ATOMIC_RELAXED, __HIP_MEMORY_SCOPE_AGENT);
    if (__all(skip || v != 0u)) break;
    ++spins;
    if ((spins & 1023u) == 0u) {
      if (spins > (1u << 20) || __hip_atomic_load(tmo, __ATOMIC_RELAXED, __HIP_MEMORY_SCOPE_AGENT) != 0u) {
        __hip_atomic_store(tmo, 1u, __ATOMIC_RELAXED, __HIP_MEMORY_SCOPE_AGENT);
        break;
      }
    }
  }
  __builtin_amdgcn_fence(__ATOMIC_ACQUIRE, "wavefront");
}
enum { F_KV, F_O, F_X1, F_HA, F_HB, F_X2 };
static constexpr unsigned ALL16 = 0xffffu, PROJ14 = 0x7f7fu;

__device__ __forceinline__ float max3f(float a, float b, float c) { float r; asm("v_max3_f32 %0, %1, %2, %3" : "=v"(r) : "v"(a), "v"(b), "v"(c)); return r; }
template <typename PF>
__device__ __forceinline__ void attn_body(const Ctx& cx, const char* kv, u32x4 qf, int w, int lane, int p, unsigned ooff, int head, PF&& pf_) {
  const int tok = lane & 15, g = lane >> 4;
  const int t0 = w & ~1;
  const char* kl = kv + t0 * 1024 + lane * 16;
  f32x4 sc[18];
  {
    u32x4 kf[2][3];
#pragma unroll
    for (int q = 0; q < 3; ++q) kf[0][q] = *(const u32x4*)(kl + q * 1024);
#pragma unroll
    for (int gi = 0; gi < 6; ++gi) {
      if (gi + 1 < 6) {
#pragma unroll
        for (int q = 0; q < 3; ++q) kf[(gi + 1) & 1][q] = *(const u32x4*)(kl + ((gi + 1) * 3 + q) * 1024);
      }
#pragma unroll
      for (int q = 0; q < 3; ++q) sc[gi * 3 + q] = mfma16(kf[gi & 1][q], qf, f32x4{0, 0, 0, 0});
      if (gi == 1 || gi == 4) pf_(gi >> 2);
      SB();
    }
  }
  asm volatile("s_nop 15\n\ts_nop 15\n\ts_nop 15" ::: "memory");
  SB();
  const int dq = 16 * (16 + w) + tok - (16 * t0 + 4 * g);
  const float NEG = -INFINITY;
#pragma unroll
  for (int i = 0; i < 18; ++i) {
    if (i < 2 || i >= 16) {
#pragma unroll
      for (int r = 0; r < 4; ++r) sc[i][r] = ((unsigned)(dq - 16 * i - r) < 256u) ? sc[i][r] : NEG;
    }
  }
  if (p < 2) {
    const int nneg = 16 - 8 * p - t0;
#pragma unroll
    for (int i = 0; i < 16; ++i)
      if (i < nneg) sc[i] = f32x4{NEG, NEG, NEG, NEG};
  }
  float m0 = max3f(sc[0][0], sc[0][1], sc[0][2]), m1 = max3f(sc[0][3], sc[1][0], sc[1][1]), m2 = max3f(sc[1][2], sc[1][3], sc[1][3]);
#pragma unroll
  for (int i = 2; i < 17; i += 3) {
    m0 = max3f(m0, sc[i][0], sc[i][1]); m1 = max3f(m1, sc[i][2], sc[i][3]);
    m2 = max3f(m2, sc[i + 1][0], sc[i + 1][1]); m0 = max3f(m0, sc[i + 1][2], sc[i + 1][3]);
    m1 = max3f(m1, sc[i + 2][0], sc[i + 2][1]); m2 = max3f(m2, sc[i + 2][2], sc[i + 2][3]);
  }
  m0 = max3f(m0, sc[17][0], sc[17][1]); m1 = max3f(m1, sc[17][2], sc[17][3]);
  float m = max3f(m0, m1, m2);
  {
    auto pp = __builtin_amdgcn_permlane16_swap(__float_as_uint(m), __float_as_uint(m), false, false);
    m = max3f(__uint_as_float(pp[0]), __uint_as_float(pp[1]), __uint_as_float(pp[1]));
    auto qq = __builtin_amdgcn_permlane32_swap(__float_as_uint(m), __float_as_uint(m), false, false);
    m = max3f(__uint_as_float(qq[0]), __uint_as_float(qq[1]), __uint_as_float(qq[1]));
  }
  const float cs = 0.18898223650461362f * 1.4426950408889634f;
  const float mc = m * cs;
  const f32x2 cs2 = {cs, cs}, mc2 = {-mc, -mc};
  u32x4 pf[9];
#pragma unroll
  for (int i = 0; i < 18; ++i) {
    f32x2 x0 = __builtin_elementwise_fma(f32x2{sc[i][0], sc[i][1]}, cs2, mc2);
    f32x2 x1 = __builtin_elementwise_fma(f32x2{sc[i][2], sc[i][3]}, cs2, mc2);
    pf[i >> 1][(i & 1) * 2 + 0] = pk_bf16(__builtin_amdgcn_exp2f(x0[0]), __builtin_amdgcn_exp2f(x0[1]));
    pf[i >> 1][(i & 1) * 2 + 1] = pk_bf16(__builtin_amdgcn_exp2f(x1[0]), __builtin_amdgcn_exp2f(x1[1]));
    if ((i & 3) == 1 && i < 16) { SB(); pf_(2 + (i >> 2)); SB(); }
  }
  const int q4 = (lane & 15) >> 2, p4 = lane & 3;
  const char* vl = kv + 24 * 1024 + t0 * 1024;
  const int vb = p4 * 256 + (((4 * g + q4) * 16 + (p4 >> 1) * 128) & 255);
  const int vofs[2] = {vb + (p4 & 1) * 8, vb + ((p4 & 1) ^ 1) * 8};
  f32x4 o[2] = {f32x4{0, 0, 0, 0}, f32x4{0, 0, 0, 0}};
  SB();
  {
    u32x4 vf[3][2];
    auto rdv = [&](int bb) {
#pragma unroll
      for (int f = 0; f < 2; ++f) vf[bb % 3][f] = tr_frag(vl + vofs[f] + (2 * bb) * 1024, vl + vofs[f] + (2 * bb + 1) * 1024);
    };
    rdv(0); rdv(1);
#pragma unroll
    for (int bb = 0; bb < 9; ++bb) {
      if (bb + 2 < 9) rdv(bb + 2);
#pragma unroll
      for (int f = 0; f < 2; ++f) o[f] = mfma16(vf[bb % 3][f], pf[bb], o[f]);
      SB();
    }
  }
  const float lsum = __shfl(o[1][0], 48 + tok);
  const float inv = __builtin_amdgcn_rcpf(lsum);
#pragma unroll
  for (int f = 0; f < 2; ++f) {
    if (f == 0 || g < 3) {
      const int k0 = DH * head + 16 * f + 4 * g;
      u32x2 st = {pk_bf16(o[f][0] * inv, o[f][1] * inv), pk_bf16(o[f][2] * inv, o[f][3] * inv)};
      st_sc1_b64(cx, (unsigned)(((k0 >> 5) * 64 + ((k0 & 31) >> 3) * 16 + tok) * 16 + (k0 & 7) * 2), ooff, st);
    }
  }
}

enum { E_X1B, E_ST0, E_ST1, E_HD0, E_HD1, E_HR0, E_HR1, E_T4A, E_T4D, E_P5P0, E_P5P1, E_P5F, E_X2D, E_COUNT };
__device__ __forceinline__ unsigned ev_arrive(char* smem, int e, int lane) {
  asm volatile("s_waitcnt lgkmcnt(0)" ::: "memory");
  unsigned old = 0;
  if (lane == 0) old = __hip_atomic_fetch_add((LAS unsigned*)(smem + LDS_EV + e * 4), 1u, __ATOMIC_RELAXED, __HIP_MEMORY_SCOPE_WORKGROUP);
  return __builtin_amdgcn_readfirstlane(old);
}
__device__ __forceinline__ void ev_post(char* smem, int e, int lane) {
  asm volatile("s_waitcnt lgkmcnt(0)" ::: "memory");
  if (lane == 0) __hip_atomic_fetch_add((LAS unsigned*)(smem + LDS_EV + e * 4), 1u, __ATOMIC_RELAXED, __HIP_MEMORY_SCOPE_WORKGROUP);
}
__device__ __forceinline__ unsigned ev_peek(char* smem, int e) {
  unsigned v = __hip_atomic_load((LAS unsigned*)(smem + LDS_EV + e * 4), __ATOMIC_RELAXED, __HIP_MEMORY_SCOPE_WORKGROUP);
  return __builtin_amdgcn_readfirstlane(v);
}
__device__ __forceinline__ void ev_wait(char* smem, int e, unsigned target) {
  while (ev_peek(smem, e) < target) __builtin_amdgcn_s_sleep(1);
  asm volatile("" ::: "memory");
  SB();
}
#define BAR() do { __builtin_amdgcn_s_barrier(); asm volatile("" ::: "memory"); SB(); } while (0)
__device__ __forceinline__ u32x2 add_res(f32x4 y, u32x2 r) {
  const unsigned y0 = pk_bf16(y[0], y[1]), y1 = pk_bf16(y[2], y[3]);
  return u32x2{pk_bf16(bf_lo(r[0]) + bf_lo(y0), bf_hi(r[0]) + bf_hi(y0)), pk_bf16(bf_lo(r[1]) + bf_lo(y1), bf_hi(r[1]) + bf_hi(y1))};
}

__global__ __launch_bounds__(512, 2) void k_main(char* __restrict__ ws_, const char* __restrict__ xin, char* __restrict__ out) {
  extern __shared__ __attribute__((aligned(16))) char smem[];
  Ctx cx;
  cx.ws = ws_;
  cx.rs = __builtin_amdgcn_make_buffer_rsrc(ws_, 0, 0x7fffffff, 0x00020000);
  const __amdgpu_buffer_rsrc_t rx = __builtin_amdgcn_make_buffer_rsrc((void*)xin, 0, S_ * D_ * 2, 0x00020000);
  const __amdgpu_buffer_rsrc_t ro = __builtin_amdgcn_make_buffer_rsrc(out, 0, S_ * D_ * 2, 0x00020000);
  if (threadIdx.x < 64) *(unsigned*)(smem + LDS_EV + threadIdx.x * 4) = 0u;
  {
    const char* pc = (const char*)(__builtin_amdgcn_s_getpc() & ~(unsigned long long)63);
    if ((blockIdx.x >> 3) < 4) {
      unsigned t = 0;
#pragma unroll
      for (int i = 0; i < 3; ++i) {
        if ((threadIdx.x & 3) == 0) t ^= *(const volatile unsigned*)(pc + i * 8192 + threadIdx.x * 16);
      }
      asm volatile("" :: "v"(t));
    }
  }

#pragma unroll 1
  for (int L = 0; L < 2; ++L) {
    int tid = threadIdx.x, b = blockIdx.x;
    asm volatile("" : "+v"(tid));
    asm volatile("" : "+s"(b));
    const int lane = tid & 63, wave = __builtin_amdgcn_readfirstlane(tid >> 6);
    const int xcd = b & 7, rr = b >> 3;
    const int p = MAPMODE == 0 ? xcd * 2 + (rr >> 4) : MAPMODE == 1 ? (xcd & 3) * 4 + (rr >> 3) : (xcd & 1) * 8 + (rr >> 2);
    const int j = MAPMODE == 0 ? (rr & 15) : MAPMODE == 1 ? (xcd >> 2) * 8 + (rr & 7) : (xcd >> 1) * 4 + (rr & 3);
    const int tok = lane & 15, g = lane >> 4;
    const bool projwg = (j & 7) < 7;
    const int qq = j >> 3, nblk = projwg ? (j & 7) : 6;
    const unsigned l16 = lane * 16;
    const unsigned r8t = (unsigned)(((g >> 1) * 16 + tok) * 16 + (g & 1) * 8);
    const unsigned r8r = (unsigned)(tok * (D_ * 2) + g * 8);
    const int tfw = p * 8 + wave;
    const unsigned M1 = (unsigned)(L + 1);
    char* const RW = smem + (L ? 0 : LDS_REG);
    char* const RA = smem + (L ? LDS_REG : 0);
    const __amdgpu_buffer_rsrc_t rsx = L ? cx.rs : rx;
    const unsigned xbase = L ? OFF_X + XT_BYTES : 0u;
    if (L == 0) {
      if (wave < 7) {
#pragma unroll
        for (int i = 0; i < 12; ++i)
          if (i % 6 >= 2) dma<0>(cx, l16, OFF_W1 + (unsigned)(j * 84 + wave * 12 + i) * 1024, smem + LDS_REG + (wave * 12 + i) * 1024);
      }
      SB();
    } else {
      if (wave == 7) poll16(cx, flag_line(L - 1, F_X2, p, j), PROJ14, lane);
      BAR();
    }
    u32x4 qf;
    {
      u32x4 a[KS];
      const unsigned va = L ? l16 : (unsigned)(tok * (D_ * 2) + g * 16), sst = L ? 1024u : 64u;
#pragma unroll
      for (int s = 0; s < KS; ++s) a[s] = __builtin_amdgcn_raw_buffer_load_b128(rsx, va, xbase + (unsigned)tfw * 14336u + s * sst, 16);
      SB();
      if (L == 0) {
        if (wave == 7) {
          const unsigned o1 = OFF_CS1 + (unsigned)(j * 96) * 4, o4 = OFF_CSGU + (unsigned)(j * 7 * 32) * 4;
          const unsigned lq = lane < 24 ? l16 : 0u, lg = lane < 56 ? l16 : 0u;
          u32x4 q0 = ld_w(cx, lq, o1), q1 = ld_w(cx, lq, o1 + 8192), g0 = ld_w(cx, lg, o4), g1 = ld_w(cx, lg, o4 + 16384);
          f32x4 qs = __builtin_bit_cast(f32x4, q0) + __builtin_bit_cast(f32x4, q1), gs = __builtin_bit_cast(f32x4, g0) + __builtin_bit_cast(f32x4, g1);
          asm volatile("" : "+v"(qs), "+v"(gs));
          if (lane < 24) *(f32x4*)(smem + LDS_TAB + 2048 + l16) = qs;
          if (lane < 56) *(f32x4*)(smem + LDS_TAB + 1024 + l16) = gs;
        } else {
#pragma unroll
          for (int i = 0; i < 12; ++i)
            if (i % 6 < 2) dma<0>(cx, l16, OFF_W1 + (unsigned)(j * 84 + wave * 12 + i) * 1024, smem + LDS_REG + (wave * 12 + i) * 1024);
          WAIT_VM(18);
        }
        BAR_LDS();
        SB();
      }
      auto w1tile = [&](int ti) { return *(const u32x4*)((ti < 56 ? RW + ti * 1024 : smem + LDS_W1B + (ti - 56) * 1024) + l16); };
      f32x2 st;
      {
        f32x4 acc[4], gram = {0, 0, 0, 0};
#pragma unroll
        for (int c = 0; c < 4; ++c) acc[c] = f32x4{0, 0, 0, 0};
        u32x4 wb[3][4];
#pragma unroll
        for (int s = 0; s < 2; ++s)
#pragma unroll
          for (int c = 0; c < 4; ++c) wb[s][c] = w1tile(s * 6 + 2 + c);
#pragma unroll
        for (int s = 0; s < KS; ++s) {
          if (s + 2 < KS) {
#pragma unroll
            for (int c = 0; c < 4; ++c) wb[(s + 2) % 3][c] = w1tile((s + 2) * 6 + 2 + c);
          }
          gram = mfma16(a[s], a[s], gram);
#pragma unroll
          for (int c = 0; c < 4; ++c) acc[c] = mfma16(wb[s % 3][c], a[s], acc[c]);
          SB();
        }
        st = stat_finish(red_add_g(g == 3 ? acc[1][0] : 0.f), gram, tok, g);
        const float mi = st[0] * st[1];
        unsigned pk[4][2];
#pragma unroll
        for (int c = 0; c < 4; ++c) {
          const f32x4 csum = *(const f32x4*)(smem + LDS_TAB + 2048 + (c + 2) * 64 + g * 16);
          float v[4];
#pragma unroll
          for (int r = 0; r < 4; ++r) v[r] = fmaf(acc[c][r], st[1], -mi * csum[r]);
          if (c == 1 && g == 3) v[0] = 0.0f;
          if (c == 3 && g == 3) v[0] = 1.0f;
          pk[c][0] = pk_bf16(v[0], v[1]);
          pk[c][1] = pk_bf16(v[2], v[3]);
        }
        u32x4 kf = {pk[0][0], pk[0][1], pk[1][0], pk[1][1]};
        u32x4 vf = (g & 1) ? u32x4{pk[3][0], pk[3][1], pk[2][0], pk[2][1]} : u32x4{pk[2][0], pk[2][1], pk[3][0], pk[3][1]};
        const unsigned vslot = (unsigned)(g * 256 + ((tok * 16 + (g >> 1) * 128) & 255));
        st_sc1(cx, l16, OFF_K + L * KV_BYTES + (unsigned)(j * 128 + tfw) * 1024, kf);
        st_sc1(cx, vslot, OFF_V + L * KV_BYTES + (unsigned)(j * 128 + tfw) * 1024, vf);
        *(u32x4*)(RA + (16 + wave) * 1024 + l16) = kf;
        *(u32x4*)(RA + (24 + 16 + wave) * 1024 + vslot) = vf;
        if (p < 2) {
          u32x4 z = {0, 0, 0, 0};
          asm volatile("" : "+v"(z));
#pragma unroll
          for (int i = 0; i < 2; ++i) {
            const int ti = wave * 2 + i;
            if (p * 8 - 16 + ti < 0) {
              *(u32x4*)(RA + ti * 1024 + l16) = z;
              *(u32x4*)(RA + (24 + ti) * 1024 + l16) = z;
            }
          }
        }
        WAIT_VM(0);
        BAR_LDS();
      }
      if (wave == 7 && lane < 2 && p + 1 + lane < 16)
        __hip_atomic_store((gu32*)(cx.ws + flag_line(L, F_KV, p + 1 + lane, j) + lane * 4), 1u, __ATOMIC_RELAXED, __HIP_MEMORY_SCOPE_AGENT);
      SB();
      {
        f32x4 acc[2] = {f32x4{0, 0, 0, 0}, f32x4{0, 0, 0, 0}};
        u32x4 wb[4][2];
#pragma unroll
        for (int s = 0; s < 3; ++s) { wb[s][0] = w1tile(s * 6); wb[s][1] = w1tile(s * 6 + 1); }
#pragma unroll
        for (int s = 0; s < KS; ++s) {
          if (s + 3 < KS) { wb[(s + 3) & 3][0] = w1tile((s + 3) * 6); wb[(s + 3) & 3][1] = w1tile((s + 3) * 6 + 1); }
          acc[0] = mfma16(wb[s & 3][0], a[s], acc[0]);
          acc[1] = mfma16(wb[s & 3][1], a[s], acc[1]);
          SB();
        }
        const float mi = st[0] * st[1];
#pragma unroll
        for (int c = 0; c < 2; ++c) {
          const f32x4 csum = *(const f32x4*)(smem + LDS_TAB + 2048 + c * 64 + g * 16);
          float v[4];
#pragma unroll
          for (int r = 0; r < 4; ++r) v[r] = fmaf(acc[c][r], st[1], -mi * csum[r]);
          qf[2 * c] = pk_bf16(v[0], v[1]);
          qf[2 * c + 1] = pk_bf16(v[2], v[3]);
        }
      }
    }
    if (wave == 7 && p >= 1) poll16(cx, flag_line(L, F_KV, p, j), p >= 2 ? 3u : 1u, lane);
    const int Fi = j * 7 + (wave < 7 ? wave : 0);
    constexpr int W4A = 6, W4E = 10;
    u32x4 w4[KS][2];
    BAR();
#pragma unroll
    for (int i = 0; i < 2; ++i) {
      const int ti = wave * 2 + i, T = p * 8 - 16 + ti;
      if (T >= 0) {
        dma<16>(cx, l16, OFF_K + L * KV_BYTES + (unsigned)(j * 128 + T) * 1024, RA + ti * 1024);
        dma<16>(cx, l16, OFF_V + L * KV_BYTES + (unsigned)(j * 128 + T) * 1024, RA + (24 + ti) * 1024);
      }
    }
    WAIT_VM(0);
    BAR();
    if (projwg) {
#pragma unroll
      for (int i = 0; i < 7; ++i) dma<0>(cx, l16, OFF_WO + (unsigned)(nblk * 56 + wave * 7 + i) * 1024, RW + (wave * 7 + i) * 1024);
    }
    SB();
    attn_body(cx, RA, qf, wave, lane, p, OFF_O + L * XT_BYTES + (unsigned)(tfw * KS) * 1024, j, [&](int s) {
      w4[s][0] = ld_w(cx, l16, OFF_WGU + (unsigned)((Fi * KS + s) * 2) * 1024);
      w4[s][1] = ld_w(cx, l16, OFF_WGU + (unsigned)((Fi * KS + s) * 2 + 1) * 1024);
    });
    WAIT_VM(0);
    BAR();
    if (wave == 7) {
      signal16(cx, L, F_O, p, j, lane);
      if (projwg) poll16(cx, flag_line(L, F_O, p, j), ALL16, lane);
    }
    if (wave < 7) {
#pragma unroll
      for (int s = W4A; s < W4E; ++s) {
        w4[s][0] = ld_w(cx, l16, OFF_WGU + (unsigned)((Fi * KS + s) * 2) * 1024);
        w4[s][1] = ld_w(cx, l16, OFF_WGU + (unsigned)((Fi * KS + s) * 2 + 1) * 1024);
      }
    } else {
#pragma unroll
      for (int s = W4A; s < W4E; ++s) { w4[s][0] = u32x4{0, 0, 0, 0}; w4[s][1] = u32x4{0, 0, 0, 0}; }
    }
    SB();
    if (projwg) {
      const int t3 = wave & 3, kh = wave >> 2;
      const int tf3 = p * 8 + qq * 4 + t3;
      const char* wlp = RW + kh * 28 * 1024 + l16;
      u32x4 wf[3][4];
#pragma unroll
      for (int i = 0; i < 2; ++i)
#pragma unroll
        for (int jj = 0; jj < 4; ++jj) wf[i][jj] = *(const u32x4*)(wlp + (i * 4 + jj) * 1024);
      u32x2 rs[2];
#pragma unroll
      for (int q = 0; q < 2; ++q) {
        const int jj = kh * 2 + q;
        const unsigned so = L ? (unsigned)(tf3 * KS + 2 * nblk + kh) * 1024 + q * 512 : (unsigned)tf3 * 14336u + nblk * 128 + jj * 32;
        rs[q] = __builtin_amdgcn_raw_buffer_load_b64(rsx, L ? r8t : r8r, xbase + so, 16);
      }
      BAR();
      u32x4 a3[7];
#pragma unroll
      for (int i = 0; i < 7; ++i) a3[i] = ld_sc1(cx, l16, OFF_O + L * XT_BYTES + (unsigned)(tf3 * KS + kh * 7 + i) * 1024);
      SB();
      f32x4 acc[4];
#pragma unroll
      for (int jj = 0; jj < 4; ++jj) acc[jj] = f32x4{0, 0, 0, 0};
#pragma unroll
      for (int i = 0; i < 7; ++i) {
        if (i + 2 < 7) {
#pragma unroll
          for (int jj = 0; jj < 4; ++jj) wf[(i + 2) % 3][jj] = *(const u32x4*)(wlp + ((i + 2) * 4 + jj) * 1024);
        }
#pragma unroll
        for (int jj = 0; jj < 4; ++jj) acc[jj] = mfma16(wf[i % 3][jj], a3[i], acc[jj]);
        SB();
      }
      char* pp3 = smem + LDS_P3 + l16;
      *(f32x4*)(pp3 + (wave * 2 + 0) * 1024) = kh ? acc[0] : acc[2];
      *(f32x4*)(pp3 + (wave * 2 + 1) * 1024) = kh ? acc[1] : acc[3];
      BAR_LDS();
#pragma unroll
      for (int q = 0; q < 2; ++q) {
        f32x4 y = (kh ? acc[2 + q] : acc[q]) + *(const f32x4*)(pp3 + ((wave ^ 4) * 2 + q) * 1024);
        st_sc1_b64(cx, r8t, OFF_X1 + L * XT_BYTES + (unsigned)(tf3 * KS + 2 * nblk + kh) * 1024 + q * 512, add_res(y, rs[q]));
      }
      WAIT_VM(0);
    }
    BAR();
    u32x4 w5[8][4];
    const unsigned w5base = OFF_WD + (unsigned)((nblk * 56 + wave * 8) * 4) * 1024;
    if (wave == 7) {
      if (projwg) signal16(cx, L, F_X1, p, j, lane);
      poll16(cx, flag_line(L, F_X1, p, j), PROJ14, lane);
    }
    if (wave < 7) {
#pragma unroll
      for (int s = W4E; s < KS; ++s) {
        w4[s][0] = ld_w(cx, l16, OFF_WGU + (unsigned)((Fi * KS + s) * 2) * 1024);
        w4[s][1] = ld_w(cx, l16, OFF_WGU + (unsigned)((Fi * KS + s) * 2 + 1) * 1024);
      }
    } else {
#pragma unroll
      for (int s = W4E; s < KS; ++s) { w4[s][0] = u32x4{0, 0, 0, 0}; w4[s][1] = u32x4{0, 0, 0, 0}; }
    }
    BAR();
#pragma unroll
    for (int i = 0; i < 7; ++i) dma<16>(cx, l16, OFF_X1 + L * XT_BYTES + (unsigned)(p * 8 * KS + wave * 7 + i) * 1024, RA + (wave * 7 + i) * 1024);
    SB();
    asm volatile("" ::: "memory");
#pragma unroll
    for (int i = 0; i < 7; ++i) dma<16>(cx, l16, OFF_X1 + L * XT_BYTES + (unsigned)((p * 8 + 4) * KS + wave * 7 + i) * 1024, RW + (wave * 7 + i) * 1024);
    WAIT_VM(7);
    BAR();
    const int tf5 = p * 8 + qq * 2;
    const int ft = wave >> 2, fj = wave & 3;
    const unsigned fso = (unsigned)(2 * nblk + (fj >> 1)) * 1024 + (fj & 1) * 512;
    u32x4 hb[8][2];
    bool hb_issued = false;
    if (wave < 7) {
#pragma unroll
      for (int hh = 0; hh < 2; ++hh) {
        const char* xt = (hh == 0 ? RA : RW) + l16;
        f32x4 acc[4][2];
#pragma unroll
        for (int t = 0; t < 4; ++t) { acc[t][0] = f32x4{0, 0, 0, 0}; acc[t][1] = f32x4{0, 0, 0, 0}; }
        u32x4 xb[3][4];
        auto rd = [&](int s) {
#pragma unroll
          for (int t = 0; t < 4; ++t) xb[s % 3][t] = *(const u32x4*)(xt + (t * KS + s) * 1024);
        };
        rd(0); rd(1);
        SB();
#pragma unroll
        for (int s = 0; s < KS; ++s) {
          if (s + 2 < KS) rd(s + 2);
#pragma unroll
          for (int t = 0; t < 4; ++t) {
            acc[t][0] = mfma16(w4[s][0], xb[s % 3][t], acc[t][0]);
            acc[t][1] = mfma16(w4[s][1], xb[s % 3][t], acc[t][1]);
          }
          SB();
          if (hh == 0 && s == 3) { WAIT_VM(0); ev_post(smem, E_X1B, lane); SB(); }
          if (hh == 0 && s >= 6) { w5[(s - 6) >> 2][(s - 6) & 3] = ld_w(cx, l16, w5base + (s - 6) * 1024); SB(); }
          if (hh == 1 && s < 12) {
#pragma unroll
            for (int q = 0; q < 2; ++q) { const int idx = 8 + s * 2 + q; w5[idx >> 2][idx & 3] = ld_w(cx, l16, w5base + idx * 1024); }
            SB();
          }
          if (hh == 1 && s == 2) {
            WAIT_VM(6);
            if (ev_arrive(smem, E_HD0, lane) == 7 * M1 - 1) signal16(cx, L, F_HA, p, j, lane);
            SB();
          }
        }
        ev_post(smem, E_T4A + hh, lane);
        ev_wait(smem, E_ST0 + hh, M1);
        const f32x4 cg = *(const f32x4*)(smem + LDS_TAB + 1024 + wave * 128 + g * 16);
        const f32x4 cu = *(const f32x4*)(smem + LDS_TAB + 1024 + wave * 128 + 64 + g * 16);
        unsigned hv[4][2];
#pragma unroll
        for (int t = 0; t < 4; ++t) {
          const f32x2 s2 = *(const f32x2*)(smem + LDS_TAB + ((hh * 4 + t) * 16 + tok) * 8);
          float hq[4];
          float nm = -s2[0], i2 = s2[1];
#pragma unroll
          for (int r = 0; r < 4; ++r) {
            float tg = fmaf(nm, cg[r], acc[t][0][r]), tu = fmaf(nm, cu[r], acc[t][1][r]);
            asm volatile("" : "+v"(tg), "+v"(tu));
            tg *= i2;
            asm volatile("" : "+v"(tg));
            hq[r] = tg * tu;
          }
          hv[t][0] = pk_bf16(hq[0], hq[1]);
          hv[t][1] = pk_bf16(hq[2], hq[3]);
        }
        const unsigned hlane = (unsigned)((g & 1) * (KSF * 1024) + ((Fi & 1) * 2 + (g >> 1)) * 256 + tok * 16);
#pragma unroll
        for (int u = 0; u < 2; ++u) {
          auto s0 = __builtin_amdgcn_permlane16_swap(hv[2 * u][0], hv[2 * u + 1][0], false, false);
          auto s1 = __builtin_amdgcn_permlane16_swap(hv[2 * u][1], hv[2 * u + 1][1], false, false);
          u32x4 o = {s0[0], s1[0], s0[1], s1[1]};
          st_sc1(cx, hlane, OFF_H + L * H_BYTES + (unsigned)((p * 8 + hh * 4 + 2 * u) * KSF + (Fi >> 1)) * 1024, o);
        }
        SB();
        if (hh == 0) { ev_wait(smem, E_X1B, 8 * M1); }
      }
      hb_issued = projwg && ev_peek(smem, E_HR0) >= M1;
      asm volatile("" ::: "memory");
      if (hb_issued) {
        const unsigned hbase = OFF_H + L * H_BYTES + (unsigned)(tf5 * KSF + wave * 8) * 1024;
#pragma unroll
        for (int i = 0; i < 8; ++i)
#pragma unroll
          for (int t = 0; t < 2; ++t) hb[i][t] = ld_sc1(cx, l16, hbase + (unsigned)(t * KSF + i) * 1024);
        SB();
        WAIT_VM(16);
      } else {
#pragma unroll
        for (int i = 0; i < 8; ++i) { hb[i][0] = u32x4{0, 0, 0, 0}; hb[i][1] = u32x4{0, 0, 0, 0}; }
        WAIT_VM(0);
      }
      if (ev_arrive(smem, E_HD1, lane) == 7 * M1 - 1) signal16(cx, L, F_HB, p, j, lane);
    } else {
#pragma unroll
      for (int i = 0; i < 32; ++i) w5[i >> 2][i & 3] = u32x4{0, 0, 0, 0};
#pragma unroll
      for (int i = 0; i < 8; ++i) { hb[i][0] = u32x4{0, 0, 0, 0}; hb[i][1] = u32x4{0, 0, 0, 0}; }
      __builtin_amdgcn_s_setprio(3);
      const u32x4 ones = {0x3f803f80u, 0x3f803f80u, 0x3f803f80u, 0x3f803f80u};
#pragma unroll
      for (int hh = 0; hh < 2; ++hh) {
        if (hh == 1) {
          WAIT_VM(0);
          ev_post(smem, E_X1B, lane);
          if (L == 0) {
#pragma unroll 1
            for (int i = 56; i < 84; i += 4) {
#pragma unroll
              for (int q = 0; q < 4; ++q) dma<0>(cx, l16, OFF_W1 + (unsigned)(j * 84 + i + q) * 1024, smem + LDS_W1B + (i - 56 + q) * 1024);
            }
          }
          ev_wait(smem, E_X1B, 8 * M1);
        }
        const char* xt = (hh == 0 ? RA : RW) + l16;
        f32x4 sm[4], gr[4];
#pragma unroll
        for (int t = 0; t < 4; ++t) { sm[t] = f32x4{0, 0, 0, 0}; gr[t] = f32x4{0, 0, 0, 0}; }
        u32x4 xb[3][4];
        auto rd = [&](int s) {
#pragma unroll
          for (int t = 0; t < 4; ++t) xb[s % 3][t] = *(const u32x4*)(xt + (t * KS + s) * 1024);
        };
        rd(0); rd(1);
        SB();
#pragma unroll
        for (int s = 0; s < KS; ++s) {
          if (s + 2 < KS) rd(s + 2);
#pragma unroll
          for (int t = 0; t < 4; ++t) {
            sm[t] = mfma16(ones, xb[s % 3][t], sm[t]);
            gr[t] = mfma16(xb[s % 3][t], xb[s % 3][t], gr[t]);
          }
          SB();
        }
#pragma unroll
        for (int t = 0; t < 4; ++t) {
          const f32x2 st = stat_finish(sm[t][0], gr[t], tok, g);
          if (g == 0) *(f32x2*)(smem + LDS_TAB + ((hh * 4 + t) * 16 + tok) * 8) = f32x2{st[0], st[1] * st[1]};
        }
        ev_post(smem, E_ST0 + hh, lane);
      }
      __builtin_amdgcn_s_setprio(0);
      if (projwg) {
        poll16(cx, flag_line(L, F_HA, p, j), ALL16, lane);
        ev_post(smem, E_HR0, lane);
      }
      if (L == 0) {
        ev_wait(smem, E_T4A, 7 * M1);
#pragma unroll 1
        for (int i = 0; i < 56; i += 4) {
#pragma unroll
          for (int q = 0; q < 4; ++q) dma<0>(cx, l16, OFF_W1 + (unsigned)(j * 84 + i + q) * 1024, RA + (i + q) * 1024);
        }
        SB();
      }
      if (projwg) {
        poll16(cx, flag_line(L, F_HB, p, j), ALL16, lane);
        ev_post(smem, E_HR1, lane);
      }
    }
    if (projwg) {
      u32x2 r5[2];
#pragma unroll
      for (int hh = 0; hh < 2; ++hh)
        r5[hh] = __builtin_amdgcn_raw_buffer_load_b64(cx.rs, r8t, OFF_X1 + L * XT_BYTES + (unsigned)((tf5 + hh * 4 + ft) * KS) * 1024 + fso, 16);
#pragma unroll
      for (int hh = 0; hh < 2; ++hh) {
        if (wave < 7) {
          if (!hb_issued) {
            ev_wait(smem, E_HR0 + hh, M1);
            const unsigned hbase = OFF_H + L * H_BYTES + (unsigned)((tf5 + hh * 4) * KSF + wave * 8) * 1024;
#pragma unroll
            for (int i = 0; i < 8; ++i)
#pragma unroll
              for (int t = 0; t < 2; ++t) hb[i][t] = ld_sc1(cx, l16, hbase + (unsigned)(t * KSF + i) * 1024);
          }
          SB();
          f32x4 acc[4][2];
#pragma unroll
          for (int jj = 0; jj < 4; ++jj) { acc[jj][0] = f32x4{0, 0, 0, 0}; acc[jj][1] = f32x4{0, 0, 0, 0}; }
#pragma unroll
          for (int i = 0; i < 8; ++i)
#pragma unroll
            for (int jj = 0; jj < 4; ++jj)
#pragma unroll
              for (int t = 0; t < 2; ++t) acc[jj][t] = mfma16(w5[i][jj], hb[i][t], acc[jj][t]);
          if (hh == 0) { ev_wait(smem, E_T4D, 7 * M1); ev_wait(smem, E_ST1, M1); } else ev_wait(smem, E_P5F, 8 * M1);
#pragma unroll
          for (int t = 0; t < 2; ++t)
#pragma unroll
            for (int jj = 0; jj < 4; ++jj) *(f32x4*)(RW + (wave * 8 + t * 4 + jj) * 1024 + l16) = acc[jj][t];
          ev_post(smem, E_P5P0 + hh, lane);
          if (hh == 0) {
            ev_wait(smem, E_HR1, M1);
            hb_issued = true;
            {
              const unsigned hbase = OFF_H + L * H_BYTES + (unsigned)((tf5 + 4) * KSF + wave * 8) * 1024;
#pragma unroll
              for (int i = 0; i < 8; ++i)
#pragma unroll
                for (int t = 0; t < 2; ++t) hb[i][t] = ld_sc1(cx, l16, hbase + (unsigned)(t * KSF + i) * 1024);
            }
            SB();
          }
        }
        ev_wait(smem, E_P5P0 + hh, 7 * M1);
        f32x4 y = *(const f32x4*)(RW + wave * 1024 + l16);
#pragma unroll
        for (int sw = 1; sw < 7; ++sw) y += *(const f32x4*)(RW + (sw * 8 + wave) * 1024 + l16);
        if (hh == 0) ev_post(smem, E_P5F, lane);
        const u32x2 o = add_res(y, r5[hh]);
        const int tf = tf5 + hh * 4 + ft;
        if (L == 0) st_sc1_b64(cx, r8t, OFF_X + XT_BYTES + (unsigned)(tf * KS) * 1024 + fso, o);
        else __builtin_amdgcn_raw_buffer_store_b64(o, ro, r8r, (unsigned)tf * 14336u + nblk * 128 + fj * 32, 0);
      }
      if (L == 0) {
        WAIT_VM(0);
        if (ev_arrive(smem, E_X2D, lane) == 7) signal16(cx, L, F_X2, p, j, lane);
      }
    }
    WAIT_VM(0);
  }
}

extern "C" void kernel_launch(void* const* d_in, const int* in_sizes, int n_in, void* d_out, int out_size, void* d_ws,
                              size_t ws_size, hipStream_t stream) {
  const uint16_t* x = (const uint16_t*)d_in[0];
  const uint16_t* wqkv = (const uint16_t*)d_in[1];
  const uint16_t* wo = (const uint16_t*)d_in[2];
  const uint16_t* wg = (const uint16_t*)d_in[4];
  const uint16_t* wu = (const uint16_t*)d_in[5];
  const uint16_t* wd = (const uint16_t*)d_in[6];
  char* ws = (char*)d_ws;
  constexpr int LP = 224 * 192;
  (void)hipFuncSetAttribute((const void*)k_main, hipFuncAttributeMaxDynamicSharedMemorySize, LDS_TOTAL);
  k_prep<<<222, 512, LP, stream>>>(x, wqkv, wo, wg, wu, wd, ws);
  k_main<<<256, 512, LDS_TOTAL, stream>>>(ws, (const char*)x, (char*)d_out);
}
```

```cpp
#include <hip/hip_runtime.h>
#include <hip/hip_bf16.h>
#include <stdint.h>

typedef __attribute__((ext_vector_type(8))) short s16x8;
typedef __attribute__((ext_vector_type(4))) short s16x4;
typedef __attribute__((ext_vector_type(4))) float f32x4;
typedef __attribute__((ext_vector_type(2))) float f32x2;
typedef __attribute__((ext_vector_type(2))) __bf16 bf16x2;
typedef __attribute__((ext_vector_type(4))) unsigned u32x4;
typedef __attribute__((ext_vector_type(2))) unsigned u32x2;
#define LAS __attribute__((address_space(3)))
typedef __attribute__((address_space(1))) unsigned gu32;

static constexpr int S_ = 2048, D_ = 448, F_ = 1792, NH = 16, DH = 28;
static constexpr int KS = 14, KSF = 56;

static constexpr unsigned OFF_CNT = 0;
static constexpr unsigned OFF_TMO = 196608;
static constexpr unsigned FLAG_BYTES = 196608 + 1024;
static constexpr unsigned OFF_W1 = 262144;
static constexpr unsigned OFF_CS1 = OFF_W1 + 16 * 14 * 6 * 1024;
static constexpr unsigned OFF_WO = OFF_CS1 + 16384;
static constexpr unsigned OFF_WGU = OFF_WO + 7 * 14 * 4 * 1024;
static constexpr unsigned OFF_CSGU = OFF_WGU + 112 * 14 * 2 * 1024;
static constexpr unsigned OFF_WD = OFF_CSGU + 32768;
static constexpr unsigned XT_BYTES = 128 * 14 * 1024;
static constexpr unsigned OFF_X = OFF_WD + 7 * 56 * 4 * 1024;
static constexpr unsigned OFF_X1 = OFF_X + 2 * XT_BYTES;
static constexpr unsigned OFF_O = OFF_X1 + 2 * XT_BYTES;
static constexpr unsigned KV_BYTES = 16 * 128 * 1024;
static constexpr unsigned OFF_K = OFF_O + 2 * XT_BYTES;
static constexpr unsigned OFF_V = OFF_K + 2 * KV_BYTES;
static constexpr unsigned H_BYTES = 128 * 56 * 1024;
static constexpr unsigned OFF_H = OFF_V + 2 * KV_BYTES;

__device__ __forceinline__ float bf_lo(unsigned u) { return __uint_as_float(u << 16); }
__device__ __forceinline__ float bf_hi(unsigned u) { return __uint_as_float(u & 0xffff0000u); }
__device__ __forceinline__ unsigned pk_bf16(float a, float b) {
  f32x2 f = {a, b};
  bf16x2 r = __builtin_convertvector(f, bf16x2);
  return __builtin_bit_cast(unsigned, r);
}
__device__ __forceinline__ float rnd_bf16(float a) { return bf_lo(pk_bf16(a, 0.f)); }
__device__ __forceinline__ float red_add_g(float v) {
  auto p = __builtin_amdgcn_permlane16_swap(__float_as_uint(v), __float_as_uint(v), false, false);
  v = __uint_as_float(p[0]) + __uint_as_float(p[1]);
  auto q = __builtin_amdgcn_permlane32_swap(__float_as_uint(v), __float_as_uint(v), false, false);
  return __uint_as_float(q[0]) + __uint_as_float(q[1]);
}
__device__ __forceinline__ f32x4 mfma16(u32x4 a, u32x4 b, f32x4 c) {
  return __builtin_amdgcn_mfma_f32_16x16x32_bf16(__builtin_bit_cast(s16x8, a), __builtin_bit_cast(s16x8, b), c, 0, 0, 0);
}
__device__ __forceinline__ u32x4 tr_frag(const char* p0, const char* p1) {
  s16x4 lo = __builtin_amdgcn_ds_read_tr16_b64_v4i16((LAS s16x4*)p0);
  s16x4 hi = __builtin_amdgcn_ds_read_tr16_b64_v4i16((LAS s16x4*)p1);
  u32x2 l2 = __builtin_bit_cast(u32x2, lo), h2 = __builtin_bit_cast(u32x2, hi);
  return u32x4{l2[0], l2[1], h2[0], h2[1]};
}
#define SB() __builtin_amdgcn_sched_barrier(0)
#define WAIT_VM(n) asm volatile("s_waitcnt vmcnt(" #n ")" ::: "memory")
#define BAR_LDS() do { asm volatile("s_waitcnt lgkmcnt(0)" ::: "memory"); __builtin_amdgcn_s_barrier(); asm volatile("" ::: "memory"); } while (0)

__device__ __forceinline__ f32x2 stat_finish(float sum, f32x4 gram, int tok, int g) {
  float d = (tok & 2) ? ((tok & 1) ? gram[3] : gram[2]) : ((tok & 1) ? gram[1] : gram[0]);
  d = (g == (tok >> 2)) ? d : 0.f;
  const float sq = red_add_g(d);
  const float mean = sum * (1.0f / D_);
  const float var = fmaxf(fmaf(-mean, mean, sq * (1.0f / D_)), 0.f);
  const float mb = rnd_bf16(mean);
  const float den = rnd_bf16(__builtin_amdgcn_sqrtf(rnd_bf16(rnd_bf16(var) + 1e-5f)));
  return f32x2{mb, __builtin_amdgcn_rcpf(den)};
}

__device__ __forceinline__ void tile_block(const uint16_t* __restrict__ src, int ld, int col0, const int (&rows)[8], char* dst,
                                           int jjstride, int lane, u32x4 (&o)[4], bool consec = false) {
  const int i = lane & 15;
  u32x2 v[8];
#pragma unroll
  for (int e = 0; e < 8; ++e) v[e] = *(const u32x2*)(src + (size_t)rows[e] * ld + col0 + 4 * i);
#pragma unroll
  for (int jj = 0; jj < 4; ++jj) {
#pragma unroll
    for (int d = 0; d < 4; ++d)
      o[jj][d] = __builtin_amdgcn_perm(v[2 * d + 1][jj >> 1], v[2 * d][jj >> 1], (jj & 1) ? 0x07060302u : 0x05040100u);
    if (consec) *(u32x4*)(dst + (size_t)(i >> 2) * jjstride + ((lane >> 4) * 16 + 4 * (i & 3) + jj) * 16) = o[jj];
    else *(u32x4*)(dst + (size_t)jj * jjstride + lane * 16) = o[jj];
  }
}

__global__ __launch_bounds__(512, 2) void k_prep(const uint16_t* __restrict__ x, const uint16_t* __restrict__ wqkv,
                                                 const uint16_t* __restrict__ wo, const uint16_t* __restrict__ wg,
                                                 const uint16_t* __restrict__ wu, const uint16_t* __restrict__ wd,
                                                 char* __restrict__ ws) {
  extern __shared__ __attribute__((aligned(16))) char smem[];
  const int tid = threadIdx.x, lane = tid & 63, wave = __builtin_amdgcn_readfirstlane(tid >> 6);
  const int b = blockIdx.x;
  const int g = lane >> 4;
  const u32x4 ones = {0x3f803f80u, 0x3f803f80u, 0x3f803f80u, 0x3f803f80u};
  if (b < 112) {
    const int kh = b & 1, which = (b >> 1) & 1, blk = b >> 2;
    if (wave < 7) {
      const int s = kh * 7 + wave;
      int rows[8];
#pragma unroll
      for (int e = 0; e < 8; ++e) rows[e] = 32 * s + 8 * g + e;
      u32x4 o[4];
      tile_block(which ? wu : wg, F_, blk * 64, rows, ws + OFF_WGU + (size_t)(((blk * 4) * 14 + s) * 2 + which) * 1024,
                 14 * 2 * 1024, lane, o);
#pragma unroll
      for (int jj = 0; jj < 4; ++jj) {
        f32x4 cs = mfma16(o[jj], ones, f32x4{0, 0, 0, 0});
        if ((lane & 15) == 0) *(f32x4*)(smem + ((wave * 4 + jj) * 4 + g) * 16) = cs;
      }
    }
    __syncthreads();
    if (tid < 64) {
      float s = 0.f;
#pragma unroll
      for (int w = 0; w < 7; ++w) s += *(const float*)(smem + (w * 64 + tid) * 4);
      const int jj = tid >> 4, row = tid & 15;
      *(float*)(ws + OFF_CSGU + (size_t)kh * 16384 + (size_t)(((blk * 4 + jj) * 2 + which) * 16 + row) * 4) = s;
    }
  } else if (b < 126) {
    const int t = b - 112, nblk = t >> 1;
    if (wave < 7) {
      const int s = (t & 1) * 7 + wave;
      int rows[8];
#pragma unroll
      for (int e = 0; e < 8; ++e) rows[e] = 32 * s + 8 * g + e;
      u32x4 o[4];
      tile_block(wo, D_, nblk * 64, rows, ws + OFF_WO + (size_t)((nblk * 14 + s) * 4) * 1024, 1024, lane, o, true);
    }
  } else if (b < 182) {
    const int t = b - 126, nblk = t >> 3;
    if (wave < 7) {
      const int s = (t & 7) * 7 + wave;
      const int Fi = 2 * s + (g >> 1);
      int rows[8];
#pragma unroll
      for (int e = 0; e < 8; ++e) rows[e] = 64 * (Fi >> 2) + 16 * (2 * (g & 1) + (e >> 2)) + 4 * (e & 3) + (Fi & 3);
      u32x4 o[4];
      tile_block(wd, D_, nblk * 64, rows, ws + OFF_WD + (size_t)((nblk * 56 + s) * 4) * 1024, 1024, lane, o, true);
    }
  } else if (b < 214) {
    const int head = (b - 182) >> 1, kh = (b - 182) & 1;
    u32x2 v[11];
#pragma unroll
    for (int it = 0; it < 11; ++it) {
      const int c = it * 512 + tid;
      const int row = c / 24, grp = c - row * 24;
      const int part = grp >> 3, gi = grp & 7;
      v[it] = u32x2{grp == 15 ? 0x00003f80u : 0u, 0u};
      if (gi < 7 && row < 224) v[it] = *(const u32x2*)(wqkv + (size_t)(kh * 224 + row) * (3 * D_) + part * D_ + head * DH + gi * 4);
    }
#pragma unroll
    for (int it = 0; it < 11; ++it) {
      const int c = it * 512 + tid;
      const int row = c / 24, grp = c - row * 24;
      int pp = (grp >> 2) + ((row >> 3) & 1);
      if (pp >= 6) pp -= 6;
      if (row < 224) *(u32x2*)(smem + row * 192 + pp * 32 + (grp & 3) * 8) = v[it];
    }
    __syncthreads();
    if (wave < 6) {
      const int c = wave;
      const int q4 = (lane & 15) >> 2, p4 = lane & 3;
      int pp = c + (g & 1);
      if (pp >= 6) pp -= 6;
      const char* base = smem + (8 * g + q4) * 192 + pp * 32 + p4 * 8;
      f32x4 cs = {0, 0, 0, 0};
#pragma unroll
      for (int s = 0; s < 7; ++s) {
        u32x4 wf = tr_frag(base + s * 32 * 192, base + s * 32 * 192 + 4 * 192);
        *(u32x4*)(ws + OFF_W1 + (size_t)((head * 14 + kh * 7 + s) * 6 + c) * 1024 + lane * 16) = wf;
        cs = mfma16(wf, ones, cs);
      }
      if ((lane & 15) == 0) *(f32x4*)(ws + OFF_CS1 + (size_t)kh * 8192 + (size_t)(head * 96 + c * 16 + 4 * g) * 4) = cs;
    }
  } else {
#pragma unroll 1
    for (unsigned o = ((b - 214) * 512 + tid) * 16; o < FLAG_BYTES; o += 8 * 512 * 16) *(u32x4*)(ws + o) = u32x4{0, 0, 0, 0};
  }
}

#define MAPMODE 2
static constexpr int LDS_REG = 57344;
static constexpr int LDS_W1B = 114688;
static constexpr int LDS_P3 = 143360;
static constexpr int LDS_TAB = 159744;
static constexpr int LDS_EV = 162816;
static constexpr int LDS_TOTAL = 163840;

struct Ctx {
  __amdgpu_buffer_rsrc_t rs;
  char* ws;
};
__device__ __forceinline__ u32x4 ld_sc1(const Ctx& c, unsigned voff, unsigned soff) { return __builtin_amdgcn_raw_buffer_load_b128(c.rs, voff, soff, 16); }
__device__ __forceinline__ u32x4 ld_w(const Ctx& c, unsigned voff, unsigned soff) { return __builtin_amdgcn_raw_buffer_load_b128(c.rs, voff, soff, 0); }
__device__ __forceinline__ void st_sc1(const Ctx& c, unsigned voff, unsigned soff, u32x4 v) { __builtin_amdgcn_raw_buffer_store_b128(v, c.rs, voff, soff, 16); }
__device__ __forceinline__ void st_sc1_b64(const Ctx& c, unsigned voff, unsigned soff, u32x2 v) { __builtin_amdgcn_raw_buffer_store_b64(v, c.rs, voff, soff, 16); }
template <int AUX>
__device__ __forceinline__ void dma(const Ctx& c, unsigned voff, unsigned soff, char* lds_uniform) {
  const unsigned la = __builtin_amdgcn_readfirstlane((unsigned)(uintptr_t)(LAS char*)lds_uniform);
  const unsigned so = __builtin_amdgcn_readfirstlane(soff);
  if (AUX) asm volatile("s_mov_b32 m0, %0\n\ts_nop 0\n\tbuffer_load_dwordx4 %1, %2, %3 offen sc1 lds" :: "s"(la), "v"(voff), "s"(c.rs), "s"(so) : "memory");
  else asm volatile("s_mov_b32 m0, %0\n\ts_nop 0\n\tbuffer_load_dwordx4 %1, %2, %3 offen lds" :: "s"(la), "v"(voff), "s"(c.rs), "s"(so) : "memory");
}
__device__ __forceinline__ unsigned flag_line(int L, int e, int p, int c) { return OFF_CNT + (unsigned)((((L * 6 + e) * 16 + p) * 16 + c) * 64); }
__device__ __forceinline__ void signal16(const Ctx& c, int L, int e, int p, int j, int lane) {
  if (lane < 16) __hip_atomic_store((gu32*)(c.ws + flag_line(L, e, p, lane) + j * 4), 1u, __ATOMIC_RELAXED, __HIP_MEMORY_SCOPE_AGENT);
}
__device__ __forceinline__ void poll16(const Ctx& c, unsigned line, unsigned need, int lane) {
  gu32* f = (gu32*)(c.ws + line + (lane & 15) * 4);
  gu32* tmo = (gu32*)(c.ws + OFF_TMO);
  const bool skip = ((need >> (lane & 15)) & 1u) == 0u;
  unsigned spins = 0;
  for (;;) {
    const unsigned v = __hip_atomic_load(f, __ATOMIC_RELAXED, __HIP_MEMORY_SCOPE_AGENT);
    if (__all(skip || v != 0u)) break;
    ++spins;
    if ((spins & 1023u) == 0u) {
      if (spins > (1u << 20) || __hip_atomic_load(tmo, __ATOMIC_RELAXED, __HIP_MEMORY_SCOPE_AGENT) != 0u) {
        __hip_atomic_store(tmo, 1u, __ATOMIC_RELAXED, __HIP_MEMORY_SCOPE_AGENT);
        break;
      }
    }
  }
  __builtin_amdgcn_fence(__ATOMIC_ACQUIRE, "wavefront");
}
enum { F_KV, F_O, F_X1, F_HA, F_HB, F_X2 };
static constexpr unsigned ALL16 = 0xffffu, PROJ14 = 0x7f7fu;

__device__ __forceinline__ float max3f(float a, float b, float c) { float r; asm("v_max3_f32 %0, %1, %2, %3" : "=v"(r) : "v"(a), "v"(b), "v"(c)); return r; }
template <typename PF>
__device__ __forceinline__ void attn_body(const Ctx& cx, const char* kv, u32x4 qf, int w, int lane, int p, unsigned ooff, int head, PF&& pf_) {
  const int tok = lane & 15, g = lane >> 4;
  const int t0 = w & ~1;
  const char* kl = kv + t0 * 1024 + lane * 16;
  f32x4 sc[18];
  {
    u32x4 kf[2][3];
#pragma unroll
    for (int q = 0; q < 3; ++q) kf[0][q] = *(const u32x4*)(kl + q * 1024);
#pragma unroll
    for (int gi = 0; gi < 6; ++gi) {
      if (gi + 1 < 6) {
#pragma unroll
        for (int q = 0; q < 3; ++q) kf[(gi + 1) & 1][q] = *(const u32x4*)(kl + ((gi + 1) * 3 + q) * 1024);
      }
#pragma unroll
      for (int q = 0; q < 3; ++q) sc[gi * 3 + q] = mfma16(kf[gi & 1][q], qf, f32x4{0, 0, 0, 0});
      if (gi == 1 || gi == 4) pf_(gi >> 2);
      SB();
    }
  }
  asm volatile("s_nop 15\n\ts_nop 15\n\ts_nop 15" ::: "memory");
  SB();
  const int dq = 16 * (16 + w) + tok - (16 * t0 + 4 * g);
  const float NEG = -INFINITY;
#pragma unroll
  for (int i = 0; i < 18; ++i) {
    if (i < 2 || i >= 16) {
#pragma unroll
      for (int r = 0; r < 4; ++r) sc[i][r] = ((unsigned)(dq - 16 * i - r) < 256u) ? sc[i][r] : NEG;
    }
  }
  if (p < 2) {
    const int nneg = 16 - 8 * p - t0;
#pragma unroll
    for (int i = 0; i < 16; ++i)
      if (i < nneg) sc[i] = f32x4{NEG, NEG, NEG, NEG};
  }
  float m0 = max3f(sc[0][0], sc[0][1], sc[0][2]), m1 = max3f(sc[0][3], sc[1][0], sc[1][1]), m2 = max3f(sc[1][2], sc[1][3], sc[1][3]);
#pragma unroll
  for (int i = 2; i < 17; i += 3) {
    m0 = max3f(m0, sc[i][0], sc[i][1]); m1 = max3f(m1, sc[i][2], sc[i][3]);
    m2 = max3f(m2, sc[i + 1][0], sc[i + 1][1]); m0 = max3f(m0, sc[i + 1][2], sc[i + 1][3]);
    m1 = max3f(m1, sc[i + 2][0], sc[i + 2][1]); m2 = max3f(m2, sc[i + 2][2], sc[i + 2][3]);
  }
  m0 = max3f(m0, sc[17][0], sc[17][1]); m1 = max3f(m1, sc[17][2], sc[17][3]);
  float m = max3f(m0, m1, m2);
  {
    auto pp = __builtin_amdgcn_permlane16_swap(__float_as_uint(m), __float_as_uint(m), false, false);
    m = max3f(__uint_as_float(pp[0]), __uint_as_float(pp[1]), __uint_as_float(pp[1]));
    auto qq = __builtin_amdgcn_permlane32_swap(__float_as_uint(m), __float_as_uint(m), false, false);
    m = max3f(__uint_as_float(qq[0]), __uint_as_float(qq[1]), __uint_as_float(qq[1]));
  }
  const float cs = 0.18898223650461362f * 1.4426950408889634f;
  const float mc = m * cs;
  const f32x2 cs2 = {cs, cs}, mc2 = {-mc, -mc};
  u32x4 pf[9];
#pragma unroll
  for (int i = 0; i < 18; ++i) {
    f32x2 x0 = __builtin_elementwise_fma(f32x2{sc[i][0], sc[i][1]}, cs2, mc2);
    f32x2 x1 = __builtin_elementwise_fma(f32x2{sc[i][2], sc[i][3]}, cs2, mc2);
    pf[i >> 1][(i & 1) * 2 + 0] = pk_bf16(__builtin_amdgcn_exp2f(x0[0]), __builtin_amdgcn_exp2f(x0[1]));
    pf[i >> 1][(i & 1) * 2 + 1] = pk_bf16(__builtin_amdgcn_exp2f(x1[0]), __builtin_amdgcn_exp2f(x1[1]));
    if ((i & 3) == 1 && i < 16) { SB(); pf_(2 + (i >> 2)); SB(); }
  }
  const int q4 = (lane & 15) >> 2, p4 = lane & 3;
  const char* vl = kv + 24 * 1024 + t0 * 1024;
  const int vb = p4 * 256 + (((4 * g + q4) * 16 + (p4 >> 1) * 128) & 255);
  const int vofs[2] = {vb + (p4 & 1) * 8, vb + ((p4 & 1) ^ 1) * 8};
  f32x4 o[2] = {f32x4{0, 0, 0, 0}, f32x4{0, 0, 0, 0}};
  SB();
  {
    u32x4 vf[3][2];
    auto rdv = [&](int bb) {
#pragma unroll
      for (int f = 0; f < 2; ++f) vf[bb % 3][f] = tr_frag(vl + vofs[f] + (2 * bb) * 1024, vl + vofs[f] + (2 * bb + 1) * 1024);
    };
    rdv(0); rdv(1);
#pragma unroll
    for (int bb = 0; bb < 9; ++bb) {
      if (bb + 2 < 9) rdv(bb + 2);
#pragma unroll
      for (int f = 0; f < 2; ++f) o[f] = mfma16(vf[bb % 3][f], pf[bb], o[f]);
      SB();
    }
  }
  const float lsum = __shfl(o[1][0], 48 + tok);
  const float inv = __builtin_amdgcn_rcpf(lsum);
#pragma unroll
  for (int f = 0; f < 2; ++f) {
    if (f == 0 || g < 3) {
      const int k0 = DH * head + 16 * f + 4 * g;
      u32x2 st = {pk_bf16(o[f][0] * inv, o[f][1] * inv), pk_bf16(o[f][2] * inv, o[f][3] * inv)};
      st_sc1_b64(cx, (unsigned)(((k0 >> 5) * 64 + ((k0 & 31) >> 3) * 16 + tok) * 16 + (k0 & 7) * 2), ooff, st);
    }
  }
}

enum { E_X1B, E_ST0, E_ST1, E_HD0, E_HD1, E_HR0, E_HR1, E_T4A, E_T4D, E_P5P0, E_P5P1, E_P5F, E_X2D, E_COUNT };
__device__ __forceinline__ unsigned ev_arrive(char* smem, int e, int lane) {
  asm volatile("s_waitcnt lgkmcnt(0)" ::: "memory");
  unsigned old = 0;
  if (lane == 0) old = __hip_atomic_fetch_add((LAS unsigned*)(smem + LDS_EV + e * 4), 1u, __ATOMIC_RELAXED, __HIP_MEMORY_SCOPE_WORKGROUP);
  return __builtin_amdgcn_readfirstlane(old);
}
__device__ __forceinline__ void ev_post(char* smem, int e, int lane) {
  asm volatile("s_waitcnt lgkmcnt(0)" ::: "memory");
  if (lane == 0) __hip_atomic_fetch_add((LAS unsigned*)(smem + LDS_EV + e * 4), 1u, __ATOMIC_RELAXED, __HIP_MEMORY_SCOPE_WORKGROUP);
}
__device__ __forceinline__ unsigned ev_peek(char* smem, int e) {
  unsigned v = __hip_atomic_load((LAS unsigned*)(smem + LDS_EV + e * 4), __ATOMIC_RELAXED, __HIP_MEMORY_SCOPE_WORKGROUP);
  return __builtin_amdgcn_readfirstlane(v);
}
__device__ __forceinline__ void ev_wait(char* smem, int e, unsigned target) {
  while (ev_peek(smem, e) < target) __builtin_amdgcn_s_sleep(1);
  asm volatile("" ::: "memory");
  SB();
}
#define BAR() do { __builtin_amdgcn_s_barrier(); asm volatile("" ::: "memory"); SB(); } while (0)
__device__ __forceinline__ u32x2 add_res(f32x4 y, u32x2 r) {
  const unsigned y0 = pk_bf16(y[0], y[1]), y1 = pk_bf16(y[2], y[3]);
  return u32x2{pk_bf16(bf_lo(r[0]) + bf_lo(y0), bf_hi(r[0]) + bf_hi(y0)), pk_bf16(bf_lo(r[1]) + bf_lo(y1), bf_hi(r[1]) + bf_hi(y1))};
}

__global__ __launch_bounds__(512, 2) void k_main(char* __restrict__ ws_, const char* __restrict__ xin, char* __restrict__ out) {
  extern __shared__ __attribute__((aligned(16))) char smem[];
  Ctx cx;
  cx.ws = ws_;
  cx.rs = __builtin_amdgcn_make_buffer_rsrc(ws_, 0, 0x7fffffff, 0x00020000);
  const __amdgpu_buffer_rsrc_t rx = __builtin_amdgcn_make_buffer_rsrc((void*)xin, 0, S_ * D_ * 2, 0x00020000);
  const __amdgpu_buffer_rsrc_t ro = __builtin_amdgcn_make_buffer_rsrc(out, 0, S_ * D_ * 2, 0x00020000);
  if (threadIdx.x < 64) *(unsigned*)(smem + LDS_EV + threadIdx.x * 4) = 0u;

#pragma unroll 1
  for (int L = 0; L < 2; ++L) {
    int tid = threadIdx.x, b = blockIdx.x;
    asm volatile("" : "+v"(tid));
    asm volatile("" : "+s"(b));
    const int lane = tid & 63, wave = __builtin_amdgcn_readfirstlane(tid >> 6);
    const int xcd = b & 7, rr = b >> 3;
    const int p = MAPMODE == 0 ? xcd * 2 + (rr >> 4) : MAPMODE == 1 ? (xcd & 3) * 4 + (rr >> 3) : (xcd & 1) * 8 + (rr >> 2);
    const int j = MAPMODE == 0 ? (rr & 15) : MAPMODE == 1 ? (xcd >> 2) * 8 + (rr & 7) : (xcd >> 1) * 4 + (rr & 3);
    const int tok = lane & 15, g = lane >> 4;
    const bool projwg = (j & 7) < 7;
    const int qq = j >> 3, nblk = projwg ? (j & 7) : 6;
    const unsigned l16 = lane * 16;
    const unsigned r8t = (unsigned)(((g >> 1) * 16 + tok) * 16 + (g & 1) * 8);
    const unsigned r8r = (unsigned)(tok * (D_ * 2) + g * 8);
    const int tfw = p * 8 + wave;
    const unsigned M1 = (unsigned)(L + 1);
    char* const RW = smem + (L ? 0 : LDS_REG);
    char* const RA = smem + (L ? LDS_REG : 0);
    const __amdgpu_buffer_rsrc_t rsx = L ? cx.rs : rx;
    const unsigned xbase = L ? OFF_X + XT_BYTES : 0u;
    if (L == 0) {
      if (wave < 7) {
#pragma unroll
        for (int i = 0; i < 12; ++i)
          if (i % 6 >= 2) dma<0>(cx, l16, OFF_W1 + (unsigned)(j * 84 + wave * 12 + i) * 1024, smem + LDS_REG + (wave * 12 + i) * 1024);
      }
      SB();
    } else {
      if (wave == 7) poll16(cx, flag_line(L - 1, F_X2, p, j), PROJ14, lane);
      BAR();
    }
    u32x4 qf;
    {
      u32x4 a[KS];
      const unsigned va = L ? l16 : (unsigned)(tok * (D_ * 2) + g * 16), sst = L ? 1024u : 64u;
#pragma unroll
      for (int s = 0; s < KS; ++s) a[s] = __builtin_amdgcn_raw_buffer_load_b128(rsx, va, xbase + (unsigned)tfw * 14336u + s * sst, 16);
      SB();
      if (L == 0) {
        if (wave == 7) {
          const unsigned o1 = OFF_CS1 + (unsigned)(j * 96) * 4, o4 = OFF_CSGU + (unsigned)(j * 7 * 32) * 4;
          const unsigned lq = lane < 24 ? l16 : 0u, lg = lane < 56 ? l16 : 0u;
          u32x4 q0 = ld_w(cx, lq, o1), q1 = ld_w(cx, lq, o1 + 8192), g0 = ld_w(cx, lg, o4), g1 = ld_w(cx, lg, o4 + 16384);
          f32x4 qs = __builtin_bit_cast(f32x4, q0) + __builtin_bit_cast(f32x4, q1), gs = __builtin_bit_cast(f32x4, g0) + __builtin_bit_cast(f32x4, g1);
          asm volatile("" : "+v"(qs), "+v"(gs));
          if (lane < 24) *(f32x4*)(smem + LDS_TAB + 2048 + l16) = qs;
          if (lane < 56) *(f32x4*)(smem + LDS_TAB + 1024 + l16) = gs;
        } else {
#pragma unroll
          for (int i = 0; i < 12; ++i)
            if (i % 6 < 2) dma<0>(cx, l16, OFF_W1 + (unsigned)(j * 84 + wave * 12 + i) * 1024, smem + LDS_REG + (wave * 12 + i) * 1024);
          WAIT_VM(18);
        }
        BAR_LDS();
        SB();
      }
      auto w1tile = [&](int ti) { return *(const u32x4*)((ti < 56 ? RW + ti * 1024 : smem + LDS_W1B + (ti - 56) * 1024) + l16); };
      f32x2 st;
      {
        f32x4 acc[4], gram = {0, 0, 0, 0};
#pragma unroll
        for (int c = 0; c < 4; ++c) acc[c] = f32x4{0, 0, 0, 0};
        u32x4 wb[3][4];
#pragma unroll
        for (int s = 0; s < 2; ++s)
#pragma unroll
          for (int c = 0; c < 4; ++c) wb[s][c] = w1tile(s * 6 + 2 + c);
#pragma unroll
        for (int s = 0; s < KS; ++s) {
          if (s + 2 < KS) {
#pragma unroll
            for (int c = 0; c < 4; ++c) wb[(s + 2) % 3][c] = w1tile((s + 2) * 6 + 2 + c);
          }
          gram = mfma16(a[s], a[s], gram);
#pragma unroll
          for (int c = 0; c < 4; ++c) acc[c] = mfma16(wb[s % 3][c], a[s], acc[c]);
          SB();
        }
        st = stat_finish(red_add_g(g == 3 ? acc[1][0] : 0.f), gram, tok, g);
        const float mi = st[0] * st[1];
        unsigned pk[4][2];
#pragma unroll
        for (int c = 0; c < 4; ++c) {
          const f32x4 csum = *(const f32x4*)(smem + LDS_TAB + 2048 + (c + 2) * 64 + g * 16);
          float v[4];
#pragma unroll
          for (int r = 0; r < 4; ++r) v[r] = fmaf(acc[c][r], st[1], -mi * csum[r]);
          if (c == 1 && g == 3) v[0] = 0.0f;
          if (c == 3 && g == 3) v[0] = 1.0f;
          pk[c][0] = pk_bf16(v[0], v[1]);
          pk[c][1] = pk_bf16(v[2], v[3]);
        }
        u32x4 kf = {pk[0][0], pk[0][1], pk[1][0], pk[1][1]};
        u32x4 vf = (g & 1) ? u32x4{pk[3][0], pk[3][1], pk[2][0], pk[2][1]} : u32x4{pk[2][0], pk[2][1], pk[3][0], pk[3][1]};
        const unsigned vslot = (unsigned)(g * 256 + ((tok * 16 + (g >> 1) * 128) & 255));
        st_sc1(cx, l16, OFF_K + L * KV_BYTES + (unsigned)(j * 128 + tfw) * 1024, kf);
        st_sc1(cx, vslot, OFF_V + L * KV_BYTES + (unsigned)(j * 128 + tfw) * 1024, vf);
        *(u32x4*)(RA + (16 + wave) * 1024 + l16) = kf;
        *(u32x4*)(RA + (24 + 16 + wave) * 1024 + vslot) = vf;
        if (p < 2) {
          u32x4 z = {0, 0, 0, 0};
          asm volatile("" : "+v"(z));
#pragma unroll
          for (int i = 0; i < 2; ++i) {
            const int ti = wave * 2 + i;
            if (p * 8 - 16 + ti < 0) {
              *(u32x4*)(RA + ti * 1024 + l16) = z;
              *(u32x4*)(RA + (24 + ti) * 1024 + l16) = z;
            }
          }
        }
        WAIT_VM(0);
        BAR_LDS();
      }
      if (wave == 7 && lane < 2 && p + 1 + lane < 16)
        __hip_atomic_store((gu32*)(cx.ws + flag_line(L, F_KV, p + 1 + lane, j) + lane * 4), 1u, __ATOMIC_RELAXED, __HIP_MEMORY_SCOPE_AGENT);
      SB();
      {
        f32x4 acc[2] = {f32x4{0, 0, 0, 0}, f32x4{0, 0, 0, 0}};
        u32x4 wb[4][2];
#pragma unroll
        for (int s = 0; s < 3; ++s) { wb[s][0] = w1tile(s * 6); wb[s][1] = w1tile(s * 6 + 1); }
#pragma unroll
        for (int s = 0; s < KS; ++s) {
          if (s + 3 < KS) { wb[(s + 3) & 3][0] = w1tile((s + 3) * 6); wb[(s + 3) & 3][1] = w1tile((s + 3) * 6 + 1); }
          acc[0] = mfma16(wb[s & 3][0], a[s], acc[0]);
          acc[1] = mfma16(wb[s & 3][1], a[s], acc[1]);
          SB();
        }
        const float mi = st[0] * st[1];
#pragma unroll
        for (int c = 0; c < 2; ++c) {
          const f32x4 csum = *(const f32x4*)(smem + LDS_TAB + 2048 + c * 64 + g * 16);
          float v[4];
#pragma unroll
          for (int r = 0; r < 4; ++r) v[r] = fmaf(acc[c][r], st[1], -mi * csum[r]);
          qf[2 * c] = pk_bf16(v[0], v[1]);
          qf[2 * c + 1] = pk_bf16(v[2], v[3]);
        }
      }
    }
    if (wave == 7 && p >= 1) poll16(cx, flag_line(L, F_KV, p, j), p >= 2 ? 3u : 1u, lane);
    const int Fi = j * 7 + (wave < 7 ? wave : 0);
    constexpr int W4A = 6, W4E = 10;
    u32x4 w4[KS][2];
    BAR();
#pragma unroll
    for (int i = 0; i < 2; ++i) {
      const int ti = wave * 2 + i, T = p * 8 - 16 + ti;
      if (T >= 0) {
        dma<16>(cx, l16, OFF_K + L * KV_BYTES + (unsigned)(j * 128 + T) * 1024, RA + ti * 1024);
        dma<16>(cx, l16, OFF_V + L * KV_BYTES + (unsigned)(j * 128 + T) * 1024, RA + (24 + ti) * 1024);
      }
    }
    WAIT_VM(0);
    BAR();
    if (projwg) {
#pragma unroll
      for (int i = 0; i < 7; ++i) dma<0>(cx, l16, OFF_WO + (unsigned)(nblk * 56 + wave * 7 + i) * 1024, RW + (wave * 7 + i) * 1024);
    }
    SB();
    attn_body(cx, RA, qf, wave, lane, p, OFF_O + L * XT_BYTES + (unsigned)(tfw * KS) * 1024, j, [&](int s) {
      w4[s][0] = ld_w(cx, l16, OFF_WGU + (unsigned)((Fi * KS + s) * 2) * 1024);
      w4[s][1] = ld_w(cx, l16, OFF_WGU + (unsigned)((Fi * KS + s) * 2 + 1) * 1024);
    });
    WAIT_VM(0);
    BAR();
    if (wave == 7) {
      signal16(cx, L, F_O, p, j, lane);
      if (projwg) poll16(cx, flag_line(L, F_O, p, j), ALL16, lane);
    }
    if (wave < 7) {
#pragma unroll
      for (int s = W4A; s < W4E; ++s) {
        w4[s][0] = ld_w(cx, l16, OFF_WGU + (unsigned)((Fi * KS + s) * 2) * 1024);
        w4[s][1] = ld_w(cx, l16, OFF_WGU + (unsigned)((Fi * KS + s) * 2 + 1) * 1024);
      }
    } else {
#pragma unroll
      for (int s = W4A; s < W4E; ++s) { w4[s][0] = u32x4{0, 0, 0, 0}; w4[s][1] = u32x4{0, 0, 0, 0}; }
    }
    SB();
    if (projwg) {
      const int t3 = wave & 3, kh = wave >> 2;
      const int tf3 = p * 8 + qq * 4 + t3;
      const char* wlp = RW + kh * 28 * 1024 + l16;
      u32x4 wf[3][4];
#pragma unroll
      for (int i = 0; i < 2; ++i)
#pragma unroll
        for (int jj = 0; jj < 4; ++jj) wf[i][jj] = *(const u32x4*)(wlp + (i * 4 + jj) * 1024);
      u32x2 rs[2];
#pragma unroll
      for (int q = 0; q < 2; ++q) {
        const int jj = kh * 2 + q;
        const unsigned so = L ? (unsigned)(tf3 * KS + 2 * nblk + kh) * 1024 + q * 512 : (unsigned)tf3 * 14336u + nblk * 128 + jj * 32;
        rs[q] = __builtin_amdgcn_raw_buffer_load_b64(rsx, L ? r8t : r8r, xbase + so, 16);
      }
      BAR();
      u32x4 a3[7];
#pragma unroll
      for (int i = 0; i < 7; ++i) a3[i] = ld_sc1(cx, l16, OFF_O + L * XT_BYTES + (unsigned)(tf3 * KS + kh * 7 + i) * 1024);
      SB();
      f32x4 acc[4];
#pragma unroll
      for (int jj = 0; jj < 4; ++jj) acc[jj] = f32x4{0, 0, 0, 0};
#pragma unroll
      for (int i = 0; i < 7; ++i) {
        if (i + 2 < 7) {
#pragma unroll
          for (int jj = 0; jj < 4; ++jj) wf[(i + 2) % 3][jj] = *(const u32x4*)(wlp + ((i + 2) * 4 + jj) * 1024);
        }
#pragma unroll
        for (int jj = 0; jj < 4; ++jj) acc[jj] = mfma16(wf[i % 3][jj], a3[i], acc[jj]);
        SB();
      }
      char* pp3 = smem + LDS_P3 + l16;
      *(f32x4*)(pp3 + (wave * 2 + 0) * 1024) = kh ? acc[0] : acc[2];
      *(f32x4*)(pp3 + (wave * 2 + 1) * 1024) = kh ? acc[1] : acc[3];
      BAR_LDS();
#pragma unroll
      for (int q = 0; q < 2; ++q) {
        f32x4 y = (kh ? acc[2 + q] : acc[q]) + *(const f32x4*)(pp3 + ((wave ^ 4) * 2 + q) * 1024);
        st_sc1_b64(cx, r8t, OFF_X1 + L * XT_BYTES + (unsigned)(tf3 * KS + 2 * nblk + kh) * 1024 + q * 512, add_res(y, rs[q]));
      }
      WAIT_VM(0);
    }
    BAR();
    u32x4 w5[8][4];
    const unsigned w5base = OFF_WD + (unsigned)((nblk * 56 + wave * 8) * 4) * 1024;
    if (wave == 7) {
      if (projwg) signal16(cx, L, F_X1, p, j, lane);
      poll16(cx, flag_line(L, F_X1, p, j), PROJ14, lane);
    }
    if (wave < 7) {
#pragma unroll
      for (int s = W4E; s < KS; ++s) {
        w4[s][0] = ld_w(cx, l16, OFF_WGU + (unsigned)((Fi * KS + s) * 2) * 1024);
        w4[s][1] = ld_w(cx, l16, OFF_WGU + (unsigned)((Fi * KS + s) * 2 + 1) * 1024);
      }
    } else {
#pragma unroll
      for (int s = W4E; s < KS; ++s) { w4[s][0] = u32x4{0, 0, 0, 0}; w4[s][1] = u32x4{0, 0, 0, 0}; }
    }
    BAR();
#pragma unroll
    for (int i = 0; i < 7; ++i) dma<16>(cx, l16, OFF_X1 + L * XT_BYTES + (unsigned)(p * 8 * KS + wave * 7 + i) * 1024, RA + (wave * 7 + i) * 1024);
    SB();
    asm volatile("" ::: "memory");
#pragma unroll
    for (int i = 0; i < 7; ++i) dma<16>(cx, l16, OFF_X1 + L * XT_BYTES + (unsigned)((p * 8 + 4) * KS + wave * 7 + i) * 1024, RW + (wave * 7 + i) * 1024);
    WAIT_VM(7);
    BAR();
    const int tf5 = p * 8 + qq * 2;
    const int ft = wave >> 2, fj = wave & 3;
    const unsigned fso = (unsigned)(2 * nblk + (fj >> 1)) * 1024 + (fj & 1) * 512;
    u32x4 hb[8][2];
    bool hb_issued = false;
    if (wave < 7) {
#pragma unroll
      for (int hh = 0; hh < 2; ++hh) {
        const char* xt = (hh == 0 ? RA : RW) + l16;
        f32x4 acc[4][2];
#pragma unroll
        for (int t = 0; t < 4; ++t) { acc[t][0] = f32x4{0, 0, 0, 0}; acc[t][1] = f32x4{0, 0, 0, 0}; }
        u32x4 xb[3][4];
        auto rd = [&](int s) {
#pragma unroll
          for (int t = 0; t < 4; ++t) xb[s % 3][t] = *(const u32x4*)(xt + (t * KS + s) * 1024);
        };
        rd(0); rd(1);
        SB();
#pragma unroll
        for (int s = 0; s < KS; ++s) {
          if (s + 2 < KS) rd(s + 2);
#pragma unroll
          for (int t = 0; t < 4; ++t) {
            acc[t][0] = mfma16(w4[s][0], xb[s % 3][t], acc[t][0]);
            acc[t][1] = mfma16(w4[s][1], xb[s % 3][t], acc[t][1]);
          }
          SB();
          if (hh == 0 && s == 3) { WAIT_VM(0); ev_post(smem, E_X1B, lane); SB(); }
          if (hh == 0 && s >= 6) { w5[(s - 6) >> 2][(s - 6) & 3] = ld_w(cx, l16, w5base + (s - 6) * 1024); SB(); }
          if (hh == 1 && s < 12) {
#pragma unroll
            for (int q = 0; q < 2; ++q) { const int idx = 8 + s * 2 + q; w5[idx >> 2][idx & 3] = ld_w(cx, l16, w5base + idx * 1024); }
            SB();
          }
          if (hh == 1 && s == 2) {
            WAIT_VM(6);
            if (ev_arrive(smem, E_HD0, lane) == 7 * M1 - 1) signal16(cx, L, F_HA, p, j, lane);
            SB();
          }
        }
        ev_post(smem, E_T4A + hh, lane);
        ev_wait(smem, E_ST0 + hh, M1);
        const f32x4 cg = *(const f32x4*)(smem + LDS_TAB + 1024 + wave * 128 + g * 16);
        const f32x4 cu = *(const f32x4*)(smem + LDS_TAB + 1024 + wave * 128 + 64 + g * 16);
        unsigned hv[4][2];
#pragma unroll
        for (int t = 0; t < 4; ++t) {
          const f32x2 s2 = *(const f32x2*)(smem + LDS_TAB + ((hh * 4 + t) * 16 + tok) * 8);
          float hq[4];
          float nm = -s2[0], i2 = s2[1];
#pragma unroll
          for (int r = 0; r < 4; ++r) {
            float tg = fmaf(nm, cg[r], acc[t][0][r]), tu = fmaf(nm, cu[r], acc[t][1][r]);
            asm volatile("" : "+v"(tg), "+v"(tu));
            tg *= i2;
            asm volatile("" : "+v"(tg));
            hq[r] = tg * tu;
          }
          hv[t][0] = pk_bf16(hq[0], hq[1]);
          hv[t][1] = pk_bf16(hq[2], hq[3]);
        }
        const unsigned hlane = (unsigned)((g & 1) * (KSF * 1024) + ((Fi & 1) * 2 + (g >> 1)) * 256 + tok * 16);
#pragma unroll
        for (int u = 0; u < 2; ++u) {
          auto s0 = __builtin_amdgcn_permlane16_swap(hv[2 * u][0], hv[2 * u + 1][0], false, false);
          auto s1 = __builtin_amdgcn_permlane16_swap(hv[2 * u][1], hv[2 * u + 1][1], false, false);
          u32x4 o = {s0[0], s1[0], s0[1], s1[1]};
          st_sc1(cx, hlane, OFF_H + L * H_BYTES + (unsigned)((p * 8 + hh * 4 + 2 * u) * KSF + (Fi >> 1)) * 1024, o);
        }
        SB();
        if (hh == 0) { ev_wait(smem, E_X1B, 8 * M1); }
      }
      hb_issued = projwg && ev_peek(smem, E_HR0) >= M1;
      asm volatile("" ::: "memory");
      if (hb_issued) {
        const unsigned hbase = OFF_H + L * H_BYTES + (unsigned)(tf5 * KSF + wave * 8) * 1024;
#pragma unroll
        for (int i = 0; i < 8; ++i)
#pragma unroll
          for (int t = 0; t < 2; ++t) hb[i][t] = ld_sc1(cx, l16, hbase + (unsigned)(t * KSF + i) * 1024);
        SB();
        WAIT_VM(16);
      } else {
#pragma unroll
        for (int i = 0; i < 8; ++i) { hb[i][0] = u32x4{0, 0, 0, 0}; hb[i][1] = u32x4{0, 0, 0, 0}; }
        WAIT_VM(0);
      }
      if (ev_arrive(smem, E_HD1, lane) == 7 * M1 - 1) signal16(cx, L, F_HB, p, j, lane);
    } else {
#pragma unroll
      for (int i = 0; i < 32; ++i) w5[i >> 2][i & 3] = u32x4{0, 0, 0, 0};
#pragma unroll
      for (int i = 0; i < 8; ++i) { hb[i][0] = u32x4{0, 0, 0, 0}; hb[i][1] = u32x4{0, 0, 0, 0}; }
      __builtin_amdgcn_s_setprio(3);
      const u32x4 ones = {0x3f803f80u, 0x3f803f80u, 0x3f803f80u, 0x3f803f80u};
#pragma unroll
      for (int hh = 0; hh < 2; ++hh) {
        if (hh == 1) {
          WAIT_VM(0);
          ev_post(smem, E_X1B, lane);
          if (L == 0) {
#pragma unroll 1
            for (int i = 56; i < 84; i += 4) {
#pragma unroll
              for (int q = 0; q < 4; ++q) dma<0>(cx, l16, OFF_W1 + (unsigned)(j * 84 + i + q) * 1024, smem + LDS_W1B + (i - 56 + q) * 1024);
            }
          }
          ev_wait(smem, E_X1B, 8 * M1);
        }
        const char* xt = (hh == 0 ? RA : RW) + l16;
        f32x4 sm[4], gr[4];
#pragma unroll
        for (int t = 0; t < 4; ++t) { sm[t] = f32x4{0, 0, 0, 0}; gr[t] = f32x4{0, 0, 0, 0}; }
        u32x4 xb[3][4];
        auto rd = [&](int s) {
#pragma unroll
          for (int t = 0; t < 4; ++t) xb[s % 3][t] = *(const u32x4*)(xt + (t * KS + s) * 1024);
        };
        rd(0); rd(1);
        SB();
#pragma unroll
        for (int s = 0; s < KS; ++s) {
          if (s + 2 < KS) rd(s + 2);
#pragma unroll
          for (int t = 0; t < 4; ++t) {
            sm[t] = mfma16(ones, xb[s % 3][t], sm[t]);
            gr[t] = mfma16(xb[s % 3][t], xb[s % 3][t], gr[t]);
          }
          SB();
        }
#pragma unroll
        for (int t = 0; t < 4; ++t) {
          const f32x2 st = stat_finish(sm[t][0], gr[t], tok, g);
          if (g == 0) *(f32x2*)(smem + LDS_TAB + ((hh * 4 + t) * 16 + tok) * 8) = f32x2{st[0], st[1] * st[1]};
        }
        ev_post(smem, E_ST0 + hh, lane);
      }
      __builtin_amdgcn_s_setprio(0);
      if (projwg) {
        poll16(cx, flag_line(L, F_HA, p, j), ALL16, lane);
        ev_post(smem, E_HR0, lane);
      }
      if (L == 0) {
        ev_wait(smem, E_T4A, 7 * M1);
#pragma unroll 1
        for (int i = 0; i < 56; i += 4) {
#pragma unroll
          for (int q = 0; q < 4; ++q) dma<0>(cx, l16, OFF_W1 + (unsigned)(j * 84 + i + q) * 1024, RA + (i + q) * 1024);
        }
        SB();
      }
      if (projwg) {
        poll16(cx, flag_line(L, F_HB, p, j), ALL16, lane);
        ev_post(smem, E_HR1, lane);
      }
    }
    if (projwg) {
      u32x2 r5[2];
#pragma unroll
      for (int hh = 0; hh < 2; ++hh)
        r5[hh] = __builtin_amdgcn_raw_buffer_load_b64(cx.rs, r8t, OFF_X1 + L * XT_BYTES + (unsigned)((tf5 + hh * 4 + ft) * KS) * 1024 + fso, 16);
#pragma unroll
      for (int hh = 0; hh < 2; ++hh) {
        if (wave < 7) {
          if (!hb_issued) {
            ev_wait(smem, E_HR0 + hh, M1);
            const unsigned hbase = OFF_H + L * H_BYTES + (unsigned)((tf5 + hh * 4) * KSF + wave * 8) * 1024;
#pragma unroll
            for (int i = 0; i < 8; ++i)
#pragma unroll
              for (int t = 0; t < 2; ++t) hb[i][t] = ld_sc1(cx, l16, hbase + (unsigned)(t * KSF + i) * 1024);
          }
          SB();
          f32x4 acc[4][2];
#pragma unroll
          for (int jj = 0; jj < 4; ++jj) { acc[jj][0] = f32x4{0, 0, 0, 0}; acc[jj][1] = f32x4{0, 0, 0, 0}; }
#pragma unroll
          for (int i = 0; i < 8; ++i)
#pragma unroll
            for (int jj = 0; jj < 4; ++jj)
#pragma unroll
              for (int t = 0; t < 2; ++t) acc[jj][t] = mfma16(w5[i][jj], hb[i][t], acc[jj][t]);
          if (hh == 0) { ev_wait(smem, E_T4D, 7 * M1); ev_wait(smem, E_ST1, M1); } else ev_wait(smem, E_P5F, 8 * M1);
#pragma unroll
          for (int t = 0; t < 2; ++t)
#pragma unroll
            for (int jj = 0; jj < 4; ++jj) *(f32x4*)(RW + (wave * 8 + t * 4 + jj) * 1024 + l16) = acc[jj][t];
          ev_post(smem, E_P5P0 + hh, lane);
          if (hh == 0) {
            ev_wait(smem, E_HR1, M1);
            hb_issued = true;
            {
              const unsigned hbase = OFF_H + L * H_BYTES + (unsigned)((tf5 + 4) * KSF + wave * 8) * 1024;
#pragma unroll
              for (int i = 0; i < 8; ++i)
#pragma unroll
                for (int t = 0; t < 2; ++t) hb[i][t] = ld_sc1(cx, l16, hbase + (unsigned)(t * KSF + i) * 1024);
            }
            SB();
          }
        }
        ev_wait(smem, E_P5P0 + hh, 7 * M1);
        f32x4 y = *(const f32x4*)(RW + wave * 1024 + l16);
#pragma unroll
        for (int sw = 1; sw < 7; ++sw) y += *(const f32x4*)(RW + (sw * 8 + wave) * 1024 + l16);
        if (hh == 0) ev_post(smem, E_P5F, lane);
        const u32x2 o = add_res(y, r5[hh]);
        const int tf = tf5 + hh * 4 + ft;
        if (L == 0) st_sc1_b64(cx, r8t, OFF_X + XT_BYTES + (unsigned)(tf * KS) * 1024 + fso, o);
        else __builtin_amdgcn_raw_buffer_store_b64(o, ro, r8r, (unsigned)tf * 14336u + nblk * 128 + fj * 32, 0);
      }
      if (L == 0) {
        WAIT_VM(0);
        if (ev_arrive(smem, E_X2D, lane) == 7) signal16(cx, L, F_X2, p, j, lane);
      }
    }
    WAIT_VM(0);
  }
}

extern "C" void kernel_launch(void* const* d_in, const int* in_sizes, int n_in, void* d_out, int out_size, void* d_ws,
                              size_t ws_size, hipStream_t stream) {
  const uint16_t* x = (const uint16_t*)d_in[0];
  const uint16_t* wqkv = (const uint16_t*)d_in[1];
  const uint16_t* wo = (const uint16_t*)d_in[2];
  const uint16_t* wg = (const uint16_t*)d_in[4];
  const uint16_t* wu = (const uint16_t*)d_in[5];
  const uint16_t* wd = (const uint16_t*)d_in[6];
  char* ws = (char*)d_ws;
  constexpr int LP = 224 * 192;
  (void)hipFuncSetAttribute((const void*)k_main, hipFuncAttributeMaxDynamicSharedMemorySize, LDS_TOTAL);
  k_prep<<<222, 512, LP, stream>>>(x, wqkv, wo, wg, wu, wd, ws);
  k_main<<<256, 512, LDS_TOTAL, stream>>>(ws, (const char*)x, (char*)d_out);
}
```
